```python
import torch

def _softmax(x, axis=-1):
    return torch.softmax(x, dim=axis)
def _gelu(x):
    return torch.nn.functional.gelu(x, approximate="tanh")
def _silu(x):
    return torch.nn.functional.silu(x)
def _sigmoid(x):
    return torch.sigmoid(x)
def _layernorm(x, axis=-1, eps=1e-5):
    m = x.mean(dim=axis, keepdim=True)
    v = x.var(dim=axis, keepdim=True, unbiased=False)
    return (x - m) / torch.sqrt(v + eps)
def _scatter_add(tgt, idx, src, axis=0):
    return tgt.clone().index_add_(axis, idx, src)
def _segment_sum(src, seg, n):
    out = torch.zeros((n,) + src.shape[1:], dtype=src.dtype)
    return out.index_add_(0, seg, src)
def _pad(x, pw):
    flat = tuple(v for pair in reversed(pw) for v in pair)
    return torch.nn.functional.pad(x, flat)

kernel_name = 'gated_mix__mha_block_cached__glu_gated_bilinear__decode'

def setup_inputs():

    _w = (lambda a: (lambda t: t.to(torch.bfloat16) if t.is_floating_point() else t)(torch.as_tensor(a)))
    def _A_setup():
        B, Sq, D = 1, 1, 512
        H, Sk, Dh = 32, 8192, 128
        return {
            "x": _w((lambda *s: torch.randn(*s, dtype=torch.bfloat16) * 0.05)(B, Sq, D)),
            "k_cache": _w((lambda *s: torch.randn(*s, dtype=torch.bfloat16) * 3.2)(B, H, Sk, Dh)),
            "v_cache": _w((lambda *s: torch.randn(*s, dtype=torch.bfloat16) * 0.2)(B, H, Sk, Dh)),
            "wq": _w((lambda *s: torch.randn(*s, dtype=torch.bfloat16) * 3.2)(D, H * Dh) * (D ** -0.5)),
            "wo": _w((lambda *s: torch.randn(*s, dtype=torch.bfloat16) * 0.2)(H * Dh, D) * ((H * Dh) ** -0.5)),
        }
    _A_in = _A_setup()

    def _B_setup():
        B, S, D, F = 1, 1, 512, 1376
        return {
            "x": _w((lambda *s: torch.randn(*s, dtype=torch.bfloat16) * 0.05)(B, S, D)),
            "w_gate": _w((lambda *s: torch.randn(*s, dtype=torch.bfloat16) * 0.8)(D, F) * (D ** -0.5)),
            "w_up": _w((lambda *s: torch.randn(*s, dtype=torch.bfloat16) * 0.8)(D, F) * (D ** -0.5)),
            "w_down": _w((lambda *s: torch.randn(*s, dtype=torch.bfloat16) * 0.8)(F, D) * (F ** -0.5)),
        }
    _B_in = _B_setup()

    return {
        "A_x": _A_in["x"],
        "A_k_cache": _A_in["k_cache"],
        "A_v_cache": _A_in["v_cache"],
        "A_wq": _A_in["wq"],
        "A_wo": _A_in["wo"],
        "B_x": _B_in["x"],
        "B_w_gate": _B_in["w_gate"],
        "B_w_up": _B_in["w_up"],
        "B_w_down": _B_in["w_down"],
        "gate_w": _w((lambda *s: torch.randn(*s, dtype=torch.bfloat16) * 0.1)(512) * 16.0),
    }

def reference(A_x, A_k_cache, A_v_cache, A_wq, A_wo, B_x, B_w_gate, B_w_up, B_w_down, gate_w):
    def _A_ref(x, k_cache, v_cache, wq, wo):
        B, Sq, D = x.shape
        H, Dh = 32, 128
        q = (lambda x, *d: x.permute(*d))((x @ wq).reshape(B, Sq, H, Dh), 0, 2, 1, 3)
        scores = torch.einsum("bhqd,bhkd->bhqk", q, k_cache) * (Dh ** -0.5)
        p = _softmax(scores, axis=-1)
        o = torch.einsum("bhqk,bhkd->bhqd", p, v_cache)
        return (lambda x, *d: x.permute(*d))(o, 0, 2, 1, 3).reshape(B, Sq, H * Dh) @ wo

    def _B_ref(x, w_gate, w_up, w_down):
        g = torch.einsum("bsd,df->bsf", x, w_gate)
        u = torch.einsum("bsd,df->bsf", x, w_up)
        h = g * u
        return torch.einsum("bsf,fd->bsd", h, w_down)

    x = A_x
    g = _sigmoid((x * gate_w).sum(-1, keepdims=True))
    a = _A_ref(x, A_k_cache, A_v_cache, A_wq, A_wo)
    b = _B_ref(x, B_w_gate, B_w_up, B_w_down)
    return g * a + (1 - g) * b
```

```cpp
#include <hip/hip_runtime.h>
#include <hip/hip_bf16.h>
#include <cstdint>

typedef unsigned u32x4 __attribute__((ext_vector_type(4)));
typedef __bf16 bf16x2 __attribute__((ext_vector_type(2)));
constexpr int D = 512, H = 32, SK = 8192, DH = 128, F = 1376;
constexpr int NSPLIT = 8;

__device__ __forceinline__ float bf2f(unsigned short b) { return __builtin_bit_cast(float, (unsigned)b << 16); }
__device__ __forceinline__ float lo(unsigned u) { return __builtin_bit_cast(float, u << 16); }
__device__ __forceinline__ float hi(unsigned u) { return __builtin_bit_cast(float, u & 0xffff0000u); }
__device__ __forceinline__ unsigned short f2bf(float f) { __bf16 b = (__bf16)f; return __builtin_bit_cast(unsigned short, b); }
__device__ __forceinline__ float rbf(float f) { return bf2f(f2bf(f)); }
__device__ __forceinline__ float dot2(unsigned k, unsigned q, float acc) {
  return __builtin_amdgcn_fdot2_f32_bf16(__builtin_bit_cast(bf16x2, k), __builtin_bit_cast(bf16x2, q), acc, false);
}
template <int CTRL> __device__ __forceinline__ float dpp(float x) {
  return __builtin_bit_cast(float, __builtin_amdgcn_mov_dpp(__builtin_bit_cast(int, x), CTRL, 0xf, 0xf, true));
}

__global__ void k1(const unsigned short* x, const unsigned short* wq, const unsigned short* wg, const unsigned short* wu,
                   unsigned short* q, float* hh) {
  int col = blockIdx.x * 64 + (threadIdx.x & 63);
  int w = threadIdx.x >> 6;
  __shared__ float red[2][4][64];
  if (blockIdx.x < 64) {
    float a = 0.f;
    for (int d = w * 128; d < w * 128 + 128; ++d) a += bf2f(x[d]) * bf2f(wq[(size_t)d * 4096 + col]);
    red[0][w][threadIdx.x & 63] = a;
    __syncthreads();
    if (w == 0) q[col] = f2bf(red[0][0][threadIdx.x] + red[0][1][threadIdx.x] + red[0][2][threadIdx.x] + red[0][3][threadIdx.x]);
  } else {
    col -= 4096;
    float a = 0.f, b = 0.f;
    if (col < F)
      for (int d = w * 128; d < w * 128 + 128; ++d) { float xv = bf2f(x[d]); a += xv * bf2f(wg[(size_t)d * F + col]); b += xv * bf2f(wu[(size_t)d * F + col]); }
    red[0][w][threadIdx.x & 63] = a; red[1][w][threadIdx.x & 63] = b;
    __syncthreads();
    if (w == 0 && col < F) {
      int t = threadIdx.x;
      float g = rbf(red[0][0][t] + red[0][1][t] + red[0][2][t] + red[0][3][t]);
      float u = rbf(red[1][0][t] + red[1][1][t] + red[1][2][t] + red[1][3][t]);
      hh[col] = rbf(g * u);
    }
  }
}

__global__ void __launch_bounds__(512) k2(const unsigned short* q, const u32x4* K, const u32x4* V, float* part) {
  const int b = blockIdx.x, h = b >> 3, s = b & 7;
  const int tid = threadIdx.x, lane = tid & 63, w = tid >> 6, r = lane >> 4, c = lane & 15;
  const u32x4 qv = *(const u32x4*)(q + h * DH + 8 * c);
  const size_t base = ((size_t)h * SK + s * 1024 + w * 128) * 16 + lane;
  u32x4 kv[32];
#pragma unroll
  for (int i = 0; i < 32; ++i) kv[i] = __builtin_nontemporal_load(K + base + i * 64);
  __builtin_amdgcn_sched_barrier(0);
  float sc[32];
#pragma unroll
  for (int i = 0; i < 32; ++i) {
    float d = dot2(kv[i][0], qv[0], 0.f); d = dot2(kv[i][1], qv[1], d); d = dot2(kv[i][2], qv[2], d); d = dot2(kv[i][3], qv[3], d);
    kv[i] = __builtin_nontemporal_load(V + base + i * 64);
    d += dpp<0x128>(d); d += dpp<0x124>(d); d += dpp<0x122>(d); d += dpp<0x121>(d);
    sc[i] = d * (0.08838834764831845f * 1.4426950408889634f);
    __builtin_amdgcn_sched_barrier(0);
  }
  float m = sc[0];
#pragma unroll
  for (int i = 1; i < 32; ++i) m = fmaxf(m, sc[i]);
  m = fmaxf(m, __shfl_xor(m, 16)); m = fmaxf(m, __shfl_xor(m, 32));
  float l = 0.f;
#pragma unroll
  for (int i = 0; i < 32; ++i) { sc[i] = __builtin_amdgcn_exp2f(sc[i] - m); l += sc[i]; }
  l += __shfl_xor(l, 16); l += __shfl_xor(l, 32);
  float acc[8] = {0, 0, 0, 0, 0, 0, 0, 0};
#pragma unroll
  for (int i = 0; i < 32; ++i) {
#pragma unroll
    for (int k = 0; k < 4; ++k) { acc[2 * k] = fmaf(sc[i], lo(kv[i][k]), acc[2 * k]); acc[2 * k + 1] = fmaf(sc[i], hi(kv[i][k]), acc[2 * k + 1]); }
    __builtin_amdgcn_sched_barrier(0);
  }
#pragma unroll
  for (int k = 0; k < 8; ++k) { acc[k] += __shfl_xor(acc[k], 16); acc[k] += __shfl_xor(acc[k], 32); }
  __shared__ float red[8][132];
  if (r == 0) {
#pragma unroll
    for (int k = 0; k < 8; ++k) red[w][4 + 8 * c + k] = acc[k];
    if (c == 0) { red[w][0] = m; red[w][1] = l; }
  }
  __syncthreads();
  if (tid < 130) {
    float M = red[0][0];
    for (int j = 1; j < 8; ++j) M = fmaxf(M, red[j][0]);
    float o = 0.f;
    for (int j = 0; j < 8; ++j) { float wt = __builtin_amdgcn_exp2f(red[j][0] - M); o += wt * (tid < 128 ? red[j][4 + tid] : red[j][1]); }
    if (tid == 129) o = M;
    part[b * 132 + tid] = o;
  }
}

__global__ void __launch_bounds__(256) k3(const float* part, const float* hh, const unsigned short* x, const unsigned short* gw,
                                          const unsigned short* wo, const unsigned short* wd, unsigned short* out) {
  __shared__ float o[4096];
  __shared__ float red[2][256][8];
  __shared__ float gred[256];
  const int t = threadIdx.x;
  for (int i = t; i < 4096; i += 256) {
    int h = i >> 7, d = i & 127;
    float M = -INFINITY;
    for (int s = 0; s < 8; ++s) M = fmaxf(M, part[(h * 8 + s) * 132 + 129]);
    float L = 0.f, a = 0.f;
    for (int s = 0; s < 8; ++s) { float wt = __builtin_amdgcn_exp2f(part[(h * 8 + s) * 132 + 129] - M); L += wt * part[(h * 8 + s) * 132 + 128]; a += wt * part[(h * 8 + s) * 132 + d]; }
    o[i] = rbf(a / L);
  }
  gred[t] = rbf(bf2f(x[t]) * bf2f(gw[t])) + rbf(bf2f(x[t + 256]) * bf2f(gw[t + 256]));
  __syncthreads();
  const int c0 = blockIdx.x * 8;
  float a[8] = {0, 0, 0, 0, 0, 0, 0, 0}, bb[8] = {0, 0, 0, 0, 0, 0, 0, 0};
  for (int rr = t; rr < 4096; rr += 256) {
    u32x4 wv = *(const u32x4*)(wo + (size_t)rr * D + c0);
    float cf = o[rr];
    for (int k = 0; k < 4; ++k) { a[2 * k] += cf * lo(wv[k]); a[2 * k + 1] += cf * hi(wv[k]); }
  }
  for (int rr = t; rr < F; rr += 256) {
    u32x4 wv = *(const u32x4*)(wd + (size_t)rr * D + c0);
    float cf = hh[rr];
    for (int k = 0; k < 4; ++k) { bb[2 * k] += cf * lo(wv[k]); bb[2 * k + 1] += cf * hi(wv[k]); }
  }
  for (int k = 0; k < 8; ++k) { red[0][t][k] = a[k]; red[1][t][k] = bb[k]; }
  __syncthreads();
  if (t < 8) {
    float A = 0.f, B = 0.f, G = 0.f;
    for (int j = 0; j < 256; ++j) { A += red[0][j][t]; B += red[1][j][t]; G += gred[j]; }
    G = rbf(G);
    float g = rbf(1.f / (1.f + __expf(-G)));
    A = rbf(A); B = rbf(B);
    out[c0 + t] = f2bf(rbf(g * A) + rbf(rbf(1.f - g) * B));
  }
}

extern "C" void kernel_launch(void* const* d_in, const int* in_sizes, int n_in, void* d_out, int out_size, void* d_ws, size_t ws_size,
                              hipStream_t stream) {
  const unsigned short* x = (const unsigned short*)d_in[0];
  const u32x4* K = (const u32x4*)d_in[1];
  const u32x4* V = (const u32x4*)d_in[2];
  const unsigned short* wq = (const unsigned short*)d_in[3];
  const unsigned short* wo = (const unsigned short*)d_in[4];
  const unsigned short* wg = (const unsigned short*)d_in[6];
  const unsigned short* wu = (const unsigned short*)d_in[7];
  const unsigned short* wd = (const unsigned short*)d_in[8];
  const unsigned short* gw = (const unsigned short*)d_in[9];
  unsigned short* q = (unsigned short*)d_ws;
  float* hh = (float*)((char*)d_ws + 16384);
  float* part = (float*)((char*)d_ws + 32768);
  k1<<<64 + 22, 256, 0, stream>>>(x, wq, wg, wu, q, hh);
  k2<<<256, 512, 0, stream>>>(q, K, V, part);
  k3<<<64, 256, 0, stream>>>(part, hh, x, gw, wo, wd, (unsigned short*)d_out);
}
```

```cpp
#include <hip/hip_runtime.h>
#include <hip/hip_bf16.h>
#include <cstdint>

#define KV_NT 1
#define STAMP 0

typedef unsigned u32x4 __attribute__((ext_vector_type(4)));
typedef unsigned u32x2 __attribute__((ext_vector_type(2)));
typedef float f32x2 __attribute__((ext_vector_type(2)));
typedef __bf16 bf16x2 __attribute__((ext_vector_type(2)));
#define GAS __attribute__((address_space(1)))
typedef const GAS u32x4* gptr4;

constexpr int D = 512, H = 32, SK = 8192, DH = 128, F = 1376;
constexpr int AUX_SC1 = 16, RSRC_W3 = 0x00020000;
constexpr int WS_EPOCH = 0;
constexpr int WS_ML = 4096;
constexpr int WS_HH = 8192;
constexpr int WS_ACC = 32768;
constexpr int WS_Y = WS_ACC + 32 * 64 * 8 * 16;
constexpr int WS_END = WS_Y + 256 * 64 * 16;
constexpr int WS_STAMP = 1 << 20;
constexpr unsigned SPIN_CAP = 1u << 18;

__device__ __forceinline__ float lo(unsigned u) { return __builtin_bit_cast(float, u << 16); }
__device__ __forceinline__ float hi(unsigned u) { return __builtin_bit_cast(float, u & 0xffff0000u); }
__device__ __forceinline__ float asf(unsigned u) { return __builtin_bit_cast(float, u); }
__device__ __forceinline__ unsigned asu(float f) { return __builtin_bit_cast(unsigned, f); }
__device__ __forceinline__ unsigned pkbf(float a, float b) {
  bf16x2 v = {(__bf16)a, (__bf16)b};
  return __builtin_bit_cast(unsigned, v);
}
__device__ __forceinline__ float rbf(float f) { return lo(pkbf(f, f)); }
__device__ __forceinline__ float dot2(unsigned k, unsigned q, float acc) {
  return __builtin_amdgcn_fdot2_f32_bf16(__builtin_bit_cast(bf16x2, k), __builtin_bit_cast(bf16x2, q), acc, false);
}
template <int CTRL> __device__ __forceinline__ float dpp(float x) {
  return __builtin_bit_cast(float, __builtin_amdgcn_mov_dpp(__builtin_bit_cast(int, x), CTRL, 0xf, 0xf, true));
}
constexpr int XOR1 = 0xB1, XOR2 = 0x4E, XOR7 = 0x141, ROR8 = 0x128, ROR4 = 0x124, ROR2 = 0x122, ROR1 = 0x121;
__device__ __forceinline__ float tr32(float a, float b) {
  auto r = __builtin_amdgcn_permlane32_swap(asu(a), asu(b), false, false);
  return asf(r[0]) + asf(r[1]);
}
__device__ __forceinline__ float tr16(float a, float b) {
  auto r = __builtin_amdgcn_permlane16_swap(asu(a), asu(b), false, false);
  return asf(r[0]) + asf(r[1]);
}
__device__ __forceinline__ float max32(float a) {
  auto r = __builtin_amdgcn_permlane32_swap(asu(a), asu(a), false, false);
  return fmaxf(asf(r[0]), asf(r[1]));
}
__device__ __forceinline__ float max16(float a) {
  auto r = __builtin_amdgcn_permlane16_swap(asu(a), asu(a), false, false);
  return fmaxf(asf(r[0]), asf(r[1]));
}
__device__ __forceinline__ u32x4 ldkv(gptr4 p) {
  return __builtin_nontemporal_load(p);
}
__device__ __forceinline__ void wg_barrier() {
  __builtin_amdgcn_fence(__ATOMIC_RELEASE, "workgroup");
  __builtin_amdgcn_s_barrier();
  __builtin_amdgcn_fence(__ATOMIC_ACQUIRE, "workgroup");
}
#define SB() __builtin_amdgcn_sched_barrier(0)
#define PIN8(a) asm volatile("" : "+v"(a[0]), "+v"(a[1]), "+v"(a[2]), "+v"(a[3]), "+v"(a[4]), "+v"(a[5]), "+v"(a[6]), "+v"(a[7]))
#define RELOAD() asm volatile("" ::: "memory")
#define ST(i) do { } while (0)
#define NSTAMP 10
#define STAMP_W0_FROM 7

__global__ void __launch_bounds__(512, 2)
fused_decode(const unsigned short* __restrict__ x_, const u32x4* __restrict__ K_, const u32x4* __restrict__ V_,
             const unsigned short* __restrict__ wq_, const unsigned short* __restrict__ wo_,
             const unsigned short* __restrict__ wg_, const unsigned short* __restrict__ wu_,
             const unsigned short* __restrict__ wd_, const unsigned short* __restrict__ gw_,
             unsigned* __restrict__ out_, unsigned char* ws) {
  __shared__ __attribute__((aligned(16))) float lds[8 * 128 * 2 + 8 * 64 * 2 + 64 + 8 * 16 + 32];
  float* const red = lds;
  float* const redA = lds + 1024;
  float* const redY = lds + 2048;
  unsigned* const qs = (unsigned*)(lds + 3072);
  float* const redM = lds + 3136;
  float* const sml = lds + 3264;

  const int b = blockIdx.x, h = (b & 7) * 4 + (b >> 6), s = (b >> 3) & 7;
  const int tid = threadIdx.x, lane = tid & 63, w = __builtin_amdgcn_readfirstlane(tid >> 6);
  const int r = lane >> 4, c = lane & 15;
  ST(0);
  const __amdgpu_buffer_rsrc_t rs = __builtin_amdgcn_make_buffer_rsrc((void*)ws, 0, WS_END, RSRC_W3);

  const gptr4 xg = (gptr4)x_;
  const u32x4 xa = xg[8 * w + 2 * r], xb = xg[8 * w + 2 * r + 1];
  const gptr4 wqp = (gptr4)wq_ + (size_t)(64 * w + 16 * r) * 512 + h * 16 + c;
  const size_t kvbase = ((size_t)h * SK + s * 1024 + w * 128) * 16 + lane;
  const gptr4 Kp = (gptr4)K_ + kvbase, Vp = (gptr4)V_ + kvbase;
  u32x4 wqv[16], kv[32];
#pragma unroll
  for (int i = 0; i < 16; ++i) { wqv[i] = wqp[i * 512]; kv[i] = ldkv(Kp + i * 64); }
#pragma unroll
  for (int i = 16; i < 32; ++i) kv[i] = ldkv(Kp + i * 64);
  const int chunk = 6 * h + s;
  const bool has_chunk = (s < 6) && (chunk < F / 8);
  const __amdgpu_buffer_rsrc_t rg = __builtin_amdgcn_make_buffer_rsrc((void*)wg_, 0, has_chunk ? D * F * 2 : 0, RSRC_W3);
  const __amdgpu_buffer_rsrc_t ru = __builtin_amdgcn_make_buffer_rsrc((void*)wu_, 0, has_chunk ? D * F * 2 : 0, RSRC_W3);
  const u32x4 g8 = __builtin_amdgcn_raw_buffer_load_b128(rg, tid * (F * 2) + chunk * 16, 0, 0);
  const u32x4 u8 = __builtin_amdgcn_raw_buffer_load_b128(ru, tid * (F * 2) + chunk * 16, 0, 0);
  const unsigned short xd = ((const GAS unsigned short*)x_)[tid];
  const unsigned epoch = __builtin_amdgcn_raw_buffer_load_b32(rs, WS_EPOCH, 0, AUX_SC1);
  SB();

  {
    float qa[8] = {0.f, 0.f, 0.f, 0.f, 0.f, 0.f, 0.f, 0.f};
#pragma unroll
    for (int i = 0; i < 16; ++i) {
      const unsigned xw = (i < 8) ? xa[(i & 7) >> 1] : xb[(i & 7) >> 1];
      const float xv = (i & 1) ? hi(xw) : lo(xw);
#pragma unroll
      for (int k = 0; k < 4; ++k) { qa[2 * k] = fmaf(xv, lo(wqv[i][k]), qa[2 * k]); qa[2 * k + 1] = fmaf(xv, hi(wqv[i][k]), qa[2 * k + 1]); }
    }
    float t0 = tr32(qa[0], qa[4]), t1 = tr32(qa[1], qa[5]), t2 = tr32(qa[2], qa[6]), t3 = tr32(qa[3], qa[7]);
    f32x2 u = {tr16(t0, t2), tr16(t1, t3)};
    *(f32x2*)(red + w * 128 + 2 * lane) = u;
  }
  wg_barrier();
  if (w == 0) {
    f32x2 a = *(const f32x2*)(red + 2 * lane);
#pragma unroll
    for (int j = 1; j < 8; ++j) a += *(const f32x2*)(red + j * 128 + 2 * lane);
    qs[4 * c + r] = pkbf(a[0], a[1]);
  }
  wg_barrier();
  const u32x4 qv = *(const u32x4*)(qs + 4 * c);
  ST(1);

  constexpr float C2 = 0.08838834764831845f * 1.4426950408889634f;
  float sc[32];
#pragma unroll
  for (int i = 0; i < 32; ++i) {
    float d = dot2(kv[i][0], qv[0], 0.f); d = dot2(kv[i][1], qv[1], d); d = dot2(kv[i][2], qv[2], d); d = dot2(kv[i][3], qv[3], d);
    kv[i] = ldkv(Vp + i * 64);
    d += dpp<ROR8>(d); d += dpp<ROR4>(d); d += dpp<ROR2>(d); d += dpp<ROR1>(d);
    sc[i] = d * C2;
    SB();
  }
  const int rho = lane >> 3, kap = lane & 7;
  const gptr4 wop = (gptr4)wo_ + (size_t)(h * 128 + 16 * w + 2 * rho) * 64 + s * 8 + kap;
  const u32x4 woA = wop[0], woB = wop[64];
  const __amdgpu_buffer_rsrc_t rd = __builtin_amdgcn_make_buffer_rsrc((void*)wd_, 0, w < 6 ? F * D * 2 : 0, RSRC_W3);
  const u32x4 wdv = __builtin_amdgcn_raw_buffer_load_b128(rd, ((6 * h + w) * 8 + rho) * 1024 + s * 128 + kap * 16, 0, 0);
  const u32x4 gx = xg[lane], gg = ((gptr4)gw_)[lane];
  SB();
  ST(2);

  float m = sc[0];
#pragma unroll
  for (int i = 1; i < 32; ++i) m = fmaxf(m, sc[i]);
  m = max32(max16(m));
  float l = 0.f;
#pragma unroll
  for (int i = 0; i < 32; ++i) { sc[i] = __builtin_amdgcn_exp2f(sc[i] - m); l += sc[i]; }
  l = tr16(l, l); l = tr32(l, l);
  {
    const float xv = lo(xd);
    float p[16];
#pragma unroll
    for (int k = 0; k < 4; ++k) { p[2 * k] = xv * lo(g8[k]); p[2 * k + 1] = xv * hi(g8[k]); p[8 + 2 * k] = xv * lo(u8[k]); p[9 + 2 * k] = xv * hi(u8[k]); }
#pragma unroll
    for (int k = 0; k < 8; ++k) p[k] = tr32(p[k], p[k + 8]);
#pragma unroll
    for (int k = 0; k < 4; ++k) p[k] = tr16(p[k], p[k + 4]);
#pragma unroll
    for (int k = 0; k < 2; ++k) { const float a = p[k] + dpp<ROR8>(p[k]), bb = p[k + 2] + dpp<ROR8>(p[k + 2]); p[k] = (lane & 8) ? bb : a; }
    { const float a = p[0] + dpp<XOR7>(p[0]), bb = p[1] + dpp<XOR7>(p[1]); p[0] = (lane & 4) ? bb : a; }
    p[0] += dpp<XOR2>(p[0]); p[0] += dpp<XOR1>(p[0]);
    if ((lane & 3) == 0) redM[w * 16 + (lane >> 2)] = p[0];
    if (lane == 0) *(f32x2*)(sml + 2 * w) = f32x2{m, l};
  }
  wg_barrier();
  const unsigned tag = epoch * 0x9E3779B1u + 0x7F4A7C15u;
  float fw;
  {
    const f32x2 ml = *(const f32x2*)(sml + 2 * (lane & 7));
    float M = ml[0];
    M = fmaxf(M, dpp<XOR1>(M)); M = fmaxf(M, dpp<XOR2>(M)); M = fmaxf(M, dpp<XOR7>(M));
    fw = __builtin_amdgcn_exp2f(m - M);
    if (w == 0) {
      float L = ml[1] * __builtin_amdgcn_exp2f(ml[0] - M);
      L += dpp<XOR1>(L); L += dpp<XOR2>(L); L += dpp<XOR7>(L);
      if (lane == 0) __builtin_amdgcn_raw_buffer_store_b128(u32x4{asu(M), tag, asu(L), tag}, rs, WS_ML + (h * 8 + s) * 16, 0, AUX_SC1);
    } else if (w == 1) {
      float t = redM[c];
#pragma unroll
      for (int j = 1; j < 8; ++j) t += redM[j * 16 + c];
      t = rbf(t);
      const float hv = rbf(t * dpp<ROR8>(t));
      if (lane < 8) __builtin_amdgcn_raw_buffer_store_b64(u32x2{asu(hv), tag}, rs, WS_HH + ((h * 8 + s) * 8 + lane) * 8, 0, AUX_SC1);
    }
  }
  ST(3);

  float acc[8] = {0.f, 0.f, 0.f, 0.f, 0.f, 0.f, 0.f, 0.f};
  u32x4 mlg; u32x2 hg;
  const int ml_off = WS_ML + (h * 8 + kap) * 16, hh_off = WS_HH + ((h * 8 + w) * 8 + rho) * 8;
#pragma unroll
  for (int i = 0; i < 32; ++i) {
    if (i == 24) {
      mlg = __builtin_amdgcn_raw_buffer_load_b128(rs, ml_off, 0, AUX_SC1);
      hg = __builtin_amdgcn_raw_buffer_load_b64(rs, hh_off, 0, AUX_SC1);
    }
#pragma unroll
    for (int k = 0; k < 4; ++k) { acc[2 * k] = fmaf(sc[i], lo(kv[i][k]), acc[2 * k]); acc[2 * k + 1] = fmaf(sc[i], hi(kv[i][k]), acc[2 * k + 1]); }
    PIN8(acc);
    SB();
  }
  {
    float t0 = tr32(acc[0], acc[4]), t1 = tr32(acc[1], acc[5]), t2 = tr32(acc[2], acc[6]), t3 = tr32(acc[3], acc[7]);
    f32x2 u = {tr16(t0, t2) * fw, tr16(t1, t3) * fw};
    *(f32x2*)(redA + w * 128 + 2 * lane) = u;
  }
  ST(4);
  wg_barrier();
  {
    const int jj = 8 * w + kap;
    const f32x2 a = *(const f32x2*)(redA + rho * 128 + 2 * (16 * (jj & 3) + (jj >> 2)));
    float t = tr32(a[0], a[1]);
    t = tr16(t, t); t += dpp<ROR8>(t);
    auto r2 = __builtin_amdgcn_permlane32_swap(asu(t), asu(t), false, false);
    __builtin_amdgcn_raw_buffer_store_b128(u32x4{r2[0], tag, r2[1], tag}, rs, lane < 8 ? WS_ACC + (h * 64 + jj) * 128 + s * 16 : WS_END, 0, AUX_SC1);
    SB();
  }
  ST(5);
  bool ok = true;
  if (__builtin_expect(__builtin_amdgcn_ballot_w64((((mlg[1] ^ tag) | (mlg[3] ^ tag)) | (hg[1] ^ tag)) != 0u) != 0ull, 0)) {
#pragma nounroll
    for (unsigned spins = 0;;) {
      RELOAD();
      mlg = __builtin_amdgcn_raw_buffer_load_b128(rs, ml_off, 0, AUX_SC1);
      hg = __builtin_amdgcn_raw_buffer_load_b64(rs, hh_off, 0, AUX_SC1);
      if (__builtin_amdgcn_ballot_w64((((mlg[1] ^ tag) | (mlg[3] ^ tag)) | (hg[1] ^ tag)) != 0u) == 0ull) break;
      if (++spins > SPIN_CAP) { ok = false; break; }
    }
  }
  float wt;
  {
    const float ms = asf(mlg[0]);
    float M = ms;
    M = fmaxf(M, dpp<XOR1>(M)); M = fmaxf(M, dpp<XOR2>(M)); M = fmaxf(M, dpp<XOR7>(M));
    wt = __builtin_amdgcn_exp2f(ms - M);
    float L = wt * asf(mlg[2]);
    L += dpp<XOR1>(L); L += dpp<XOR2>(L); L += dpp<XOR7>(L);
    wt *= __builtin_amdgcn_rcpf(L);
  }
  {
    const float hv = asf(hg[0]);
    float y[8];
#pragma unroll
    for (int k = 0; k < 4; ++k) { y[2 * k] = hv * lo(wdv[k]); y[2 * k + 1] = hv * hi(wdv[k]); }
    float t0 = tr32(y[0], y[4]), t1 = tr32(y[1], y[5]), t2 = tr32(y[2], y[6]), t3 = tr32(y[3], y[7]);
    float u0 = tr16(t0, t2), u1 = tr16(t1, t3);
    u0 += dpp<ROR8>(u0); u1 += dpp<ROR8>(u1);
    redY[512 + w * 64 + lane] = (lane & 8) ? u1 : u0;
  }
  float gate = 0.f;
  if (w == 0) {
    float t = 0.f;
#pragma unroll
    for (int k = 0; k < 4; ++k) { t += rbf(lo(gx[k]) * lo(gg[k])); t += rbf(hi(gx[k]) * hi(gg[k])); }
    t += dpp<XOR1>(t); t += dpp<XOR2>(t); t += dpp<XOR7>(t); t += dpp<ROR8>(t);
    t = tr16(t, t); t = tr32(t, t);
    t = rbf(t);
    gate = rbf(1.f / (1.f + __expf(-t)));
  }
  float wa[8], wb[8];
#pragma unroll
  for (int k = 0; k < 4; ++k) { wa[2 * k] = lo(woA[k]); wa[2 * k + 1] = hi(woA[k]); wb[2 * k] = lo(woB[k]); wb[2 * k + 1] = hi(woB[k]); }
  PIN8(wa); PIN8(wb);
  asm volatile("" : "+v"(wt), "+v"(gate));
  SB();
  {
    const int off = WS_ACC + (h * 64 + 8 * w) * 128 + lane * 16;
    u32x4 g;
#pragma nounroll
    for (unsigned spins = 0;;) {
      RELOAD();
      g = __builtin_amdgcn_raw_buffer_load_b128(rs, off, 0, AUX_SC1);
      if (__builtin_amdgcn_ballot_w64(((g[1] ^ tag) | (g[3] ^ tag)) != 0u) == 0ull) break;
      if (++spins > SPIN_CAP) { ok = false; break; }
    }
    ST(6);
    float o0 = wt * asf(g[0]), o1 = wt * asf(g[2]);
    o0 += dpp<XOR1>(o0); o1 += dpp<XOR1>(o1); o0 += dpp<XOR2>(o0); o1 += dpp<XOR2>(o1); o0 += dpp<XOR7>(o0); o1 += dpp<XOR7>(o1);
    const unsigned op = pkbf(o0, o1);
    o0 = lo(op); o1 = hi(op);
    float y[8];
#pragma unroll
    for (int k = 0; k < 8; ++k) y[k] = fmaf(o1, wb[k], o0 * wa[k]);
    float t0 = tr32(y[0], y[4]), t1 = tr32(y[1], y[5]), t2 = tr32(y[2], y[6]), t3 = tr32(y[3], y[7]);
    float u0 = tr16(t0, t2), u1 = tr16(t1, t3);
    u0 += dpp<ROR8>(u0); u1 += dpp<ROR8>(u1);
    float yv = (lane & 8) ? u1 : u0;
    if (!ok) yv = __builtin_nanf("");
    redY[w * 64 + lane] = yv;
  }
  wg_barrier();
  if (w >= 2) return;
  {
    float t = redY[w * 512 + lane];
#pragma unroll
    for (int j = 1; j < 8; ++j) t += redY[w * 512 + j * 64 + lane];
    const int col = 8 * kap + ((lane >> 5) << 2) + (((lane >> 4) & 1) << 1) + ((lane >> 3) & 1);
    __builtin_amdgcn_raw_buffer_store_b64(u32x2{asu(t), tag}, rs, WS_Y + (32 * s + (col >> 1)) * 1024 + (32 * w + h) * 16 + (col & 1) * 8, 0, AUX_SC1);
  }
  if (w == 1) return;
  ST(7);
  {
    const int cp = 32 * s + h;
    const int off = WS_Y + cp * 1024 + lane * 16;
    u32x4 g;
#pragma nounroll
    for (unsigned spins = 0;;) {
      RELOAD();
      g = __builtin_amdgcn_raw_buffer_load_b128(rs, off, 0, AUX_SC1);
      if (__builtin_amdgcn_ballot_w64(((g[1] ^ tag) | (g[3] ^ tag)) != 0u) == 0ull) break;
      if (++spins > SPIN_CAP) { ok = false; break; }
    }
    ST(8);
    float v0 = asf(g[0]), v1 = asf(g[2]);
    v0 += dpp<XOR1>(v0); v1 += dpp<XOR1>(v1); v0 += dpp<XOR2>(v0); v1 += dpp<XOR2>(v1);
    v0 += dpp<XOR7>(v0); v1 += dpp<XOR7>(v1); v0 += dpp<ROR8>(v0); v1 += dpp<ROR8>(v1);
    float t = tr16(v0, v1);
    t = rbf(t);
    const float coef = (lane < 32) ? gate : rbf(1.f - gate);
    t = rbf(coef * t);
    t = tr32(t, t);
    const float c0 = asf(__builtin_amdgcn_readlane(asu(t), 0)), c1 = asf(__builtin_amdgcn_readlane(asu(t), 16));
    unsigned o = pkbf(c0, c1);
    if (!ok) o = 0x7fc07fc0u;
    if (lane == 0) {
      ((GAS unsigned*)out_)[cp] = o;
      if (b == 0) __builtin_amdgcn_raw_buffer_store_b32(tag, rs, WS_EPOCH, 0, AUX_SC1);
    }
  }
  ST(9);
}

extern "C" void kernel_launch(void* const* d_in, const int* in_sizes, int n_in, void* d_out, int out_size, void* d_ws, size_t ws_size,
                              hipStream_t stream) {
  fused_decode<<<256, 512, 0, stream>>>((const unsigned short*)d_in[0], (const u32x4*)d_in[1], (const u32x4*)d_in[2],
                                        (const unsigned short*)d_in[3], (const unsigned short*)d_in[4], (const unsigned short*)d_in[6],
                                        (const unsigned short*)d_in[7], (const unsigned short*)d_in[8], (const unsigned short*)d_in[9],
                                        (unsigned*)d_out, (unsigned char*)d_ws);
}
```

```cpp
#include <hip/hip_runtime.h>
#include <hip/hip_bf16.h>
#include <cstdint>

#define KV_NT 1
#define STAMP 0

typedef unsigned u32x4 __attribute__((ext_vector_type(4)));
typedef unsigned u32x2 __attribute__((ext_vector_type(2)));
typedef float f32x2 __attribute__((ext_vector_type(2)));
typedef __bf16 bf16x2 __attribute__((ext_vector_type(2)));
#define GAS __attribute__((address_space(1)))
typedef const GAS u32x4* gptr4;

constexpr int D = 512, H = 32, SK = 8192, DH = 128, F = 1376;
constexpr int AUX_SC1 = 16, RSRC_W3 = 0x00020000;
constexpr int WS_EPOCH = 0;
constexpr int WS_ML = 4096;
constexpr int WS_HH = 8192;
constexpr int WS_ACC = 32768;
constexpr int WS_Y = WS_ACC + 32 * 64 * 8 * 16;
constexpr int WS_END = WS_Y + 256 * 64 * 16;
constexpr int WS_STAMP = 1 << 20;
constexpr unsigned SPIN_CAP = 1u << 18;

__device__ __forceinline__ float lo(unsigned u) { return __builtin_bit_cast(float, u << 16); }
__device__ __forceinline__ float hi(unsigned u) { return __builtin_bit_cast(float, u & 0xffff0000u); }
__device__ __forceinline__ float asf(unsigned u) { return __builtin_bit_cast(float, u); }
__device__ __forceinline__ unsigned asu(float f) { return __builtin_bit_cast(unsigned, f); }
__device__ __forceinline__ unsigned pkbf(float a, float b) {
  bf16x2 v = {(__bf16)a, (__bf16)b};
  return __builtin_bit_cast(unsigned, v);
}
__device__ __forceinline__ float rbf(float f) { return lo(pkbf(f, f)); }
__device__ __forceinline__ float dot2(unsigned k, unsigned q, float acc) {
  return __builtin_amdgcn_fdot2_f32_bf16(__builtin_bit_cast(bf16x2, k), __builtin_bit_cast(bf16x2, q), acc, false);
}
template <int CTRL> __device__ __forceinline__ float dpp(float x) {
  return __builtin_bit_cast(float, __builtin_amdgcn_mov_dpp(__builtin_bit_cast(int, x), CTRL, 0xf, 0xf, true));
}
constexpr int XOR1 = 0xB1, XOR2 = 0x4E, XOR7 = 0x141, ROR8 = 0x128, ROR4 = 0x124, ROR2 = 0x122, ROR1 = 0x121;
__device__ __forceinline__ float tr32(float a, float b) {
  auto r = __builtin_amdgcn_permlane32_swap(asu(a), asu(b), false, false);
  return asf(r[0]) + asf(r[1]);
}
__device__ __forceinline__ float tr16(float a, float b) {
  auto r = __builtin_amdgcn_permlane16_swap(asu(a), asu(b), false, false);
  return asf(r[0]) + asf(r[1]);
}
__device__ __forceinline__ float max32(float a) {
  auto r = __builtin_amdgcn_permlane32_swap(asu(a), asu(a), false, false);
  return fmaxf(asf(r[0]), asf(r[1]));
}
__device__ __forceinline__ float max16(float a) {
  auto r = __builtin_amdgcn_permlane16_swap(asu(a), asu(a), false, false);
  return fmaxf(asf(r[0]), asf(r[1]));
}
__device__ __forceinline__ u32x4 ldkv(gptr4 p) {
  return __builtin_nontemporal_load(p);
}
__device__ __forceinline__ void wg_barrier() {
  __builtin_amdgcn_fence(__ATOMIC_RELEASE, "workgroup");
  __builtin_amdgcn_s_barrier();
  __builtin_amdgcn_fence(__ATOMIC_ACQUIRE, "workgroup");
}
#define SB() __builtin_amdgcn_sched_barrier(0)
#define PIN8(a) asm volatile("" : "+v"(a[0]), "+v"(a[1]), "+v"(a[2]), "+v"(a[3]), "+v"(a[4]), "+v"(a[5]), "+v"(a[6]), "+v"(a[7]))
#define RELOAD() asm volatile("" ::: "memory")
#define ST(i) do { } while (0)
#define NSTAMP 10
#define STAMP_W0_FROM 7

__global__ void __launch_bounds__(512, 2)
fused_decode(const unsigned short* __restrict__ x_, const u32x4* __restrict__ K_, const u32x4* __restrict__ V_,
             const unsigned short* __restrict__ wq_, const unsigned short* __restrict__ wo_,
             const unsigned short* __restrict__ wg_, const unsigned short* __restrict__ wu_,
             const unsigned short* __restrict__ wd_, const unsigned short* __restrict__ gw_,
             unsigned* __restrict__ out_, unsigned char* ws) {
  unsigned long long tstart;
  asm volatile("s_memrealtime %0\n\ts_waitcnt lgkmcnt(0)" : "=s"(tstart) :: "memory");
  __builtin_amdgcn_sched_barrier(0);
  __shared__ __attribute__((aligned(16))) float lds[8 * 128 * 2 + 8 * 64 * 2 + 64 + 8 * 16 + 32];
  float* const red = lds;
  float* const redA = lds + 1024;
  float* const redY = lds + 2048;
  unsigned* const qs = (unsigned*)(lds + 3072);
  float* const redM = lds + 3136;
  float* const sml = lds + 3264;

  const int b = blockIdx.x, h = (b & 7) * 4 + (b >> 6), s = (b >> 3) & 7;
  const int tid = threadIdx.x, lane = tid & 63, w = __builtin_amdgcn_readfirstlane(tid >> 6);
  const int r = lane >> 4, c = lane & 15;
  ST(0);
  const __amdgpu_buffer_rsrc_t rs = __builtin_amdgcn_make_buffer_rsrc((void*)ws, 0, WS_END, RSRC_W3);

  const gptr4 xg = (gptr4)x_;
  const u32x4 xa = xg[8 * w + 2 * r], xb = xg[8 * w + 2 * r + 1];
  const gptr4 wqp = (gptr4)wq_ + (size_t)(64 * w + 16 * r) * 512 + h * 16 + c;
  const size_t kvbase = ((size_t)h * SK + s * 1024 + w * 128) * 16 + lane;
  const gptr4 Kp = (gptr4)K_ + kvbase, Vp = (gptr4)V_ + kvbase;
  u32x4 wqv[16], kv[32];
#pragma unroll
  for (int i = 0; i < 16; ++i) { wqv[i] = wqp[i * 512]; kv[i] = ldkv(Kp + i * 64); }
#pragma unroll
  for (int i = 16; i < 32; ++i) kv[i] = ldkv(Kp + i * 64);
  const int chunk = 6 * h + s;
  const bool has_chunk = (s < 6) && (chunk < F / 8);
  const __amdgpu_buffer_rsrc_t rg = __builtin_amdgcn_make_buffer_rsrc((void*)wg_, 0, has_chunk ? D * F * 2 : 0, RSRC_W3);
  const __amdgpu_buffer_rsrc_t ru = __builtin_amdgcn_make_buffer_rsrc((void*)wu_, 0, has_chunk ? D * F * 2 : 0, RSRC_W3);
  const u32x4 g8 = __builtin_amdgcn_raw_buffer_load_b128(rg, tid * (F * 2) + chunk * 16, 0, 0);
  const u32x4 u8 = __builtin_amdgcn_raw_buffer_load_b128(ru, tid * (F * 2) + chunk * 16, 0, 0);
  const unsigned short xd = ((const GAS unsigned short*)x_)[tid];
  const unsigned epoch = __builtin_amdgcn_raw_buffer_load_b32(rs, WS_EPOCH, 0, AUX_SC1);
  SB();

  {
    float qa[8] = {0.f, 0.f, 0.f, 0.f, 0.f, 0.f, 0.f, 0.f};
#pragma unroll
    for (int i = 0; i < 16; ++i) {
      const unsigned xw = (i < 8) ? xa[(i & 7) >> 1] : xb[(i & 7) >> 1];
      const float xv = (i & 1) ? hi(xw) : lo(xw);
#pragma unroll
      for (int k = 0; k < 4; ++k) { qa[2 * k] = fmaf(xv, lo(wqv[i][k]), qa[2 * k]); qa[2 * k + 1] = fmaf(xv, hi(wqv[i][k]), qa[2 * k + 1]); }
    }
    float t0 = tr32(qa[0], qa[4]), t1 = tr32(qa[1], qa[5]), t2 = tr32(qa[2], qa[6]), t3 = tr32(qa[3], qa[7]);
    f32x2 u = {tr16(t0, t2), tr16(t1, t3)};
    *(f32x2*)(red + w * 128 + 2 * lane) = u;
  }
  wg_barrier();
  if (w == 0) {
    f32x2 a = *(const f32x2*)(red + 2 * lane);
#pragma unroll
    for (int j = 1; j < 8; ++j) a += *(const f32x2*)(red + j * 128 + 2 * lane);
    qs[4 * c + r] = pkbf(a[0], a[1]);
  }
  wg_barrier();
  const u32x4 qv = *(const u32x4*)(qs + 4 * c);
  ST(1);

  constexpr float C2 = 0.08838834764831845f * 1.4426950408889634f;
  float sc[32];
#pragma unroll
  for (int i = 0; i < 32; ++i) {
    float d = dot2(kv[i][0], qv[0], 0.f); d = dot2(kv[i][1], qv[1], d); d = dot2(kv[i][2], qv[2], d); d = dot2(kv[i][3], qv[3], d);
    kv[i] = ldkv(Vp + i * 64);
    d += dpp<ROR8>(d); d += dpp<ROR4>(d); d += dpp<ROR2>(d); d += dpp<ROR1>(d);
    sc[i] = d * C2;
    SB();
  }
  const int rho = lane >> 3, kap = lane & 7;
  const gptr4 wop = (gptr4)wo_ + (size_t)(h * 128 + 16 * w + 2 * rho) * 64 + s * 8 + kap;
  const u32x4 woA = wop[0], woB = wop[64];
  const __amdgpu_buffer_rsrc_t rd = __builtin_amdgcn_make_buffer_rsrc((void*)wd_, 0, w < 6 ? F * D * 2 : 0, RSRC_W3);
  const u32x4 wdv = __builtin_amdgcn_raw_buffer_load_b128(rd, ((6 * h + w) * 8 + rho) * 1024 + s * 128 + kap * 16, 0, 0);
  const u32x4 gx = xg[lane], gg = ((gptr4)gw_)[lane];
  SB();
  ST(2);

  float m = sc[0];
#pragma unroll
  for (int i = 1; i < 32; ++i) m = fmaxf(m, sc[i]);
  m = max32(max16(m));
  float l = 0.f;
#pragma unroll
  for (int i = 0; i < 32; ++i) { sc[i] = __builtin_amdgcn_exp2f(sc[i] - m); l += sc[i]; }
  l = tr16(l, l); l = tr32(l, l);
  {
    const float xv = lo(xd);
    float p[16];
#pragma unroll
    for (int k = 0; k < 4; ++k) { p[2 * k] = xv * lo(g8[k]); p[2 * k + 1] = xv * hi(g8[k]); p[8 + 2 * k] = xv * lo(u8[k]); p[9 + 2 * k] = xv * hi(u8[k]); }
#pragma unroll
    for (int k = 0; k < 8; ++k) p[k] = tr32(p[k], p[k + 8]);
#pragma unroll
    for (int k = 0; k < 4; ++k) p[k] = tr16(p[k], p[k + 4]);
#pragma unroll
    for (int k = 0; k < 2; ++k) { const float a = p[k] + dpp<ROR8>(p[k]), bb = p[k + 2] + dpp<ROR8>(p[k + 2]); p[k] = (lane & 8) ? bb : a; }
    { const float a = p[0] + dpp<XOR7>(p[0]), bb = p[1] + dpp<XOR7>(p[1]); p[0] = (lane & 4) ? bb : a; }
    p[0] += dpp<XOR2>(p[0]); p[0] += dpp<XOR1>(p[0]);
    if ((lane & 3) == 0) redM[w * 16 + (lane >> 2)] = p[0];
    if (lane == 0) *(f32x2*)(sml + 2 * w) = f32x2{m, l};
  }
  wg_barrier();
  const unsigned tag = epoch * 0x9E3779B1u + 0x7F4A7C15u;
  float fw;
  {
    const f32x2 ml = *(const f32x2*)(sml + 2 * (lane & 7));
    float M = ml[0];
    M = fmaxf(M, dpp<XOR1>(M)); M = fmaxf(M, dpp<XOR2>(M)); M = fmaxf(M, dpp<XOR7>(M));
    fw = __builtin_amdgcn_exp2f(m - M);
    if (w == 0) {
      float L = ml[1] * __builtin_amdgcn_exp2f(ml[0] - M);
      L += dpp<XOR1>(L); L += dpp<XOR2>(L); L += dpp<XOR7>(L);
      if (lane == 0) __builtin_amdgcn_raw_buffer_store_b128(u32x4{asu(M), tag, asu(L), tag}, rs, WS_ML + (h * 8 + s) * 16, 0, AUX_SC1);
    } else if (w == 1) {
      float t = redM[c];
#pragma unroll
      for (int j = 1; j < 8; ++j) t += redM[j * 16 + c];
      t = rbf(t);
      const float hv = rbf(t * dpp<ROR8>(t));
      if (lane < 8) __builtin_amdgcn_raw_buffer_store_b64(u32x2{asu(hv), tag}, rs, WS_HH + ((h * 8 + s) * 8 + lane) * 8, 0, AUX_SC1);
    }
  }
  ST(3);

  float acc[8] = {0.f, 0.f, 0.f, 0.f, 0.f, 0.f, 0.f, 0.f};
  u32x4 mlg; u32x2 hg;
  const int ml_off = WS_ML + (h * 8 + kap) * 16, hh_off = WS_HH + ((h * 8 + w) * 8 + rho) * 8;
#pragma unroll
  for (int i = 0; i < 32; ++i) {
    if (i == 24) {
      mlg = __builtin_amdgcn_raw_buffer_load_b128(rs, ml_off, 0, AUX_SC1);
      hg = __builtin_amdgcn_raw_buffer_load_b64(rs, hh_off, 0, AUX_SC1);
    }
#pragma unroll
    for (int k = 0; k < 4; ++k) { acc[2 * k] = fmaf(sc[i], lo(kv[i][k]), acc[2 * k]); acc[2 * k + 1] = fmaf(sc[i], hi(kv[i][k]), acc[2 * k + 1]); }
    PIN8(acc);
    SB();
  }
  {
    float t0 = tr32(acc[0], acc[4]), t1 = tr32(acc[1], acc[5]), t2 = tr32(acc[2], acc[6]), t3 = tr32(acc[3], acc[7]);
    f32x2 u = {tr16(t0, t2) * fw, tr16(t1, t3) * fw};
    *(f32x2*)(redA + w * 128 + 2 * lane) = u;
  }
  ST(4);
  wg_barrier();
  {
    const int jj = 8 * w + kap;
    const f32x2 a = *(const f32x2*)(redA + rho * 128 + 2 * (16 * (jj & 3) + (jj >> 2)));
    float t = tr32(a[0], a[1]);
    t = tr16(t, t); t += dpp<ROR8>(t);
    auto r2 = __builtin_amdgcn_permlane32_swap(asu(t), asu(t), false, false);
    __builtin_amdgcn_raw_buffer_store_b128(u32x4{r2[0], tag, r2[1], tag}, rs, lane < 8 ? WS_ACC + (h * 64 + jj) * 128 + s * 16 : WS_END, 0, AUX_SC1);
    SB();
  }
  ST(5);
  bool ok = true;
  if (__builtin_expect(__builtin_amdgcn_ballot_w64((((mlg[1] ^ tag) | (mlg[3] ^ tag)) | (hg[1] ^ tag)) != 0u) != 0ull, 0)) {
#pragma nounroll
    for (unsigned spins = 0;;) {
      RELOAD();
      mlg = __builtin_amdgcn_raw_buffer_load_b128(rs, ml_off, 0, AUX_SC1);
      hg = __builtin_amdgcn_raw_buffer_load_b64(rs, hh_off, 0, AUX_SC1);
      if (__builtin_amdgcn_ballot_w64((((mlg[1] ^ tag) | (mlg[3] ^ tag)) | (hg[1] ^ tag)) != 0u) == 0ull) break;
      if (++spins > SPIN_CAP) { ok = false; break; }
    }
  }
  float wt;
  {
    const float ms = asf(mlg[0]);
    float M = ms;
    M = fmaxf(M, dpp<XOR1>(M)); M = fmaxf(M, dpp<XOR2>(M)); M = fmaxf(M, dpp<XOR7>(M));
    wt = __builtin_amdgcn_exp2f(ms - M);
    float L = wt * asf(mlg[2]);
    L += dpp<XOR1>(L); L += dpp<XOR2>(L); L += dpp<XOR7>(L);
    wt *= __builtin_amdgcn_rcpf(L);
  }
  {
    const float hv = asf(hg[0]);
    float y[8];
#pragma unroll
    for (int k = 0; k < 4; ++k) { y[2 * k] = hv * lo(wdv[k]); y[2 * k + 1] = hv * hi(wdv[k]); }
    float t0 = tr32(y[0], y[4]), t1 = tr32(y[1], y[5]), t2 = tr32(y[2], y[6]), t3 = tr32(y[3], y[7]);
    float u0 = tr16(t0, t2), u1 = tr16(t1, t3);
    u0 += dpp<ROR8>(u0); u1 += dpp<ROR8>(u1);
    redY[512 + w * 64 + lane] = (lane & 8) ? u1 : u0;
  }
  float gate = 0.f;
  if (w == 0) {
    float t = 0.f;
#pragma unroll
    for (int k = 0; k < 4; ++k) { t += rbf(lo(gx[k]) * lo(gg[k])); t += rbf(hi(gx[k]) * hi(gg[k])); }
    t += dpp<XOR1>(t); t += dpp<XOR2>(t); t += dpp<XOR7>(t); t += dpp<ROR8>(t);
    t = tr16(t, t); t = tr32(t, t);
    t = rbf(t);
    gate = rbf(1.f / (1.f + __expf(-t)));
  }
  float wa[8], wb[8];
#pragma unroll
  for (int k = 0; k < 4; ++k) { wa[2 * k] = lo(woA[k]); wa[2 * k + 1] = hi(woA[k]); wb[2 * k] = lo(woB[k]); wb[2 * k + 1] = hi(woB[k]); }
  PIN8(wa); PIN8(wb);
  asm volatile("" : "+v"(wt), "+v"(gate));
  SB();
  {
    const int off = WS_ACC + (h * 64 + 8 * w) * 128 + lane * 16;
    u32x4 g;
#pragma nounroll
    for (unsigned spins = 0;;) {
      RELOAD();
      g = __builtin_amdgcn_raw_buffer_load_b128(rs, off, 0, AUX_SC1);
      if (__builtin_amdgcn_ballot_w64(((g[1] ^ tag) | (g[3] ^ tag)) != 0u) == 0ull) break;
      if (++spins > SPIN_CAP) { ok = false; break; }
    }
    ST(6);
    float o0 = wt * asf(g[0]), o1 = wt * asf(g[2]);
    o0 += dpp<XOR1>(o0); o1 += dpp<XOR1>(o1); o0 += dpp<XOR2>(o0); o1 += dpp<XOR2>(o1); o0 += dpp<XOR7>(o0); o1 += dpp<XOR7>(o1);
    const unsigned op = pkbf(o0, o1);
    o0 = lo(op); o1 = hi(op);
    float y[8];
#pragma unroll
    for (int k = 0; k < 8; ++k) y[k] = fmaf(o1, wb[k], o0 * wa[k]);
    float t0 = tr32(y[0], y[4]), t1 = tr32(y[1], y[5]), t2 = tr32(y[2], y[6]), t3 = tr32(y[3], y[7]);
    float u0 = tr16(t0, t2), u1 = tr16(t1, t3);
    u0 += dpp<ROR8>(u0); u1 += dpp<ROR8>(u1);
    float yv = (lane & 8) ? u1 : u0;
    if (!ok) yv = __builtin_nanf("");
    redY[w * 64 + lane] = yv;
  }
  wg_barrier();
  if (w >= 2) return;
  {
    float t = redY[w * 512 + lane];
#pragma unroll
    for (int j = 1; j < 8; ++j) t += redY[w * 512 + j * 64 + lane];
    const int col = 8 * kap + ((lane >> 5) << 2) + (((lane >> 4) & 1) << 1) + ((lane >> 3) & 1);
    __builtin_amdgcn_raw_buffer_store_b64(u32x2{asu(t), tag}, rs, WS_Y + (32 * s + (col >> 1)) * 1024 + (32 * w + h) * 16 + (col & 1) * 8, 0, AUX_SC1);
  }
  if (w == 1) return;
  ST(7);
  {
    const int cp = 32 * s + h;
    const int off = WS_Y + cp * 1024 + lane * 16;
    u32x4 g;
#pragma nounroll
    for (unsigned spins = 0;;) {
      RELOAD();
      g = __builtin_amdgcn_raw_buffer_load_b128(rs, off, 0, AUX_SC1);
      if (__builtin_amdgcn_ballot_w64(((g[1] ^ tag) | (g[3] ^ tag)) != 0u) == 0ull) break;
      if (++spins > SPIN_CAP) { ok = false; break; }
    }
    ST(8);
    float v0 = asf(g[0]), v1 = asf(g[2]);
    v0 += dpp<XOR1>(v0); v1 += dpp<XOR1>(v1); v0 += dpp<XOR2>(v0); v1 += dpp<XOR2>(v1);
    v0 += dpp<XOR7>(v0); v1 += dpp<XOR7>(v1); v0 += dpp<ROR8>(v0); v1 += dpp<ROR8>(v1);
    float t = tr16(v0, v1);
    t = rbf(t);
    const float coef = (lane < 32) ? gate : rbf(1.f - gate);
    t = rbf(coef * t);
    t = tr32(t, t);
    const float c0 = asf(__builtin_amdgcn_readlane(asu(t), 0)), c1 = asf(__builtin_amdgcn_readlane(asu(t), 16));
    unsigned o = pkbf(c0, c1);
    if (!ok) o = 0x7fc07fc0u;
    if (lane == 0) {
      ((GAS unsigned*)out_)[cp] = o;
      if (b == 0) __builtin_amdgcn_raw_buffer_store_b32(tag, rs, WS_EPOCH, 0, AUX_SC1);
    }
  }
  ST(9);
  while (__builtin_amdgcn_s_memrealtime() < tstart + 6000ull) __builtin_amdgcn_s_sleep(1);
}

extern "C" void kernel_launch(void* const* d_in, const int* in_sizes, int n_in, void* d_out, int out_size, void* d_ws, size_t ws_size,
                              hipStream_t stream) {
  fused_decode<<<256, 512, 0, stream>>>((const unsigned short*)d_in[0], (const u32x4*)d_in[1], (const u32x4*)d_in[2],
                                        (const unsigned short*)d_in[3], (const unsigned short*)d_in[4], (const unsigned short*)d_in[6],
                                        (const unsigned short*)d_in[7], (const unsigned short*)d_in[8], (const unsigned short*)d_in[9],
                                        (unsigned*)d_out, (unsigned char*)d_ws);
}
```

```cpp
#include <hip/hip_runtime.h>
#include <hip/hip_bf16.h>
#include <cstdint>

#define KV_NT 1
#define STAMP 0

typedef unsigned u32x4 __attribute__((ext_vector_type(4)));
typedef unsigned u32x2 __attribute__((ext_vector_type(2)));
typedef float f32x2 __attribute__((ext_vector_type(2)));
typedef __bf16 bf16x2 __attribute__((ext_vector_type(2)));
#define GAS __attribute__((address_space(1)))
typedef const GAS u32x4* gptr4;

constexpr int D = 512, H = 32, SK = 8192, DH = 128, F = 1376;
constexpr int AUX_SC1 = 16, RSRC_W3 = 0x00020000;
constexpr int WS_EPOCH = 0;
constexpr int WS_ML = 4096;
constexpr int WS_HH = 8192;
constexpr int WS_ACC = 32768;
constexpr int WS_Y = WS_ACC + 32 * 64 * 8 * 16;
constexpr int WS_END = WS_Y + 256 * 64 * 16;
constexpr int WS_STAMP = 1 << 20;
constexpr unsigned SPIN_CAP = 1u << 18;

__device__ __forceinline__ float lo(unsigned u) { return __builtin_bit_cast(float, u << 16); }
__device__ __forceinline__ float hi(unsigned u) { return __builtin_bit_cast(float, u & 0xffff0000u); }
__device__ __forceinline__ float asf(unsigned u) { return __builtin_bit_cast(float, u); }
__device__ __forceinline__ unsigned asu(float f) { return __builtin_bit_cast(unsigned, f); }
__device__ __forceinline__ unsigned pkbf(float a, float b) {
  bf16x2 v = {(__bf16)a, (__bf16)b};
  return __builtin_bit_cast(unsigned, v);
}
__device__ __forceinline__ float rbf(float f) { return lo(pkbf(f, f)); }
__device__ __forceinline__ float dot2(unsigned k, unsigned q, float acc) {
  return __builtin_amdgcn_fdot2_f32_bf16(__builtin_bit_cast(bf16x2, k), __builtin_bit_cast(bf16x2, q), acc, false);
}
template <int CTRL> __device__ __forceinline__ float dpp(float x) {
  return __builtin_bit_cast(float, __builtin_amdgcn_mov_dpp(__builtin_bit_cast(int, x), CTRL, 0xf, 0xf, true));
}
constexpr int XOR1 = 0xB1, XOR2 = 0x4E, XOR7 = 0x141, ROR8 = 0x128, ROR4 = 0x124, ROR2 = 0x122, ROR1 = 0x121;
__device__ __forceinline__ float tr32(float a, float b) {
  auto r = __builtin_amdgcn_permlane32_swap(asu(a), asu(b), false, false);
  return asf(r[0]) + asf(r[1]);
}
__device__ __forceinline__ float tr16(float a, float b) {
  auto r = __builtin_amdgcn_permlane16_swap(asu(a), asu(b), false, false);
  return asf(r[0]) + asf(r[1]);
}
__device__ __forceinline__ float max32(float a) {
  auto r = __builtin_amdgcn_permlane32_swap(asu(a), asu(a), false, false);
  return fmaxf(asf(r[0]), asf(r[1]));
}
__device__ __forceinline__ float max16(float a) {
  auto r = __builtin_amdgcn_permlane16_swap(asu(a), asu(a), false, false);
  return fmaxf(asf(r[0]), asf(r[1]));
}
__device__ __forceinline__ u32x4 ldkv(gptr4 p) {
  return __builtin_nontemporal_load(p);
}
__device__ __forceinline__ void wg_barrier() {
  __builtin_amdgcn_fence(__ATOMIC_RELEASE, "workgroup");
  __builtin_amdgcn_s_barrier();
  __builtin_amdgcn_fence(__ATOMIC_ACQUIRE, "workgroup");
}
#define SB() __builtin_amdgcn_sched_barrier(0)
#define PIN8(a) asm volatile("" : "+v"(a[0]), "+v"(a[1]), "+v"(a[2]), "+v"(a[3]), "+v"(a[4]), "+v"(a[5]), "+v"(a[6]), "+v"(a[7]))
#define RELOAD() asm volatile("" ::: "memory")
#define ST(i) do { } while (0)
#define NSTAMP 10
#define STAMP_W0_FROM 7

__global__ void __launch_bounds__(512, 2)
fused_decode(const unsigned short* __restrict__ a0, const u32x4* __restrict__ a1, const u32x4* __restrict__ a2,
             const unsigned short* __restrict__ a3, const unsigned short* __restrict__ a4,
             const unsigned short* __restrict__ a5, const unsigned short* __restrict__ a6,
             const unsigned short* __restrict__ a7, const unsigned short* __restrict__ a8,
             unsigned* __restrict__ a9, unsigned char* a10) {
  typedef const __attribute__((address_space(4))) unsigned long long* kaptr;
  kaptr ka = (kaptr)__builtin_amdgcn_kernarg_segment_ptr();
  unsigned long long tstart;
  asm volatile("s_memrealtime %0\n\ts_waitcnt lgkmcnt(0)" : "=s"(tstart), "+s"(ka) :: "memory");
  const unsigned short* x_ = (const unsigned short*)ka[0]; const u32x4* K_ = (const u32x4*)ka[1]; const u32x4* V_ = (const u32x4*)ka[2];
  const unsigned short* wq_ = (const unsigned short*)ka[3]; const unsigned short* wo_ = (const unsigned short*)ka[4];
  const unsigned short* wg_ = (const unsigned short*)ka[5]; const unsigned short* wu_ = (const unsigned short*)ka[6];
  const unsigned short* wd_ = (const unsigned short*)ka[7]; const unsigned short* gw_ = (const unsigned short*)ka[8];
  unsigned* out_ = (unsigned*)ka[9]; unsigned char* ws = (unsigned char*)ka[10];
  __shared__ __attribute__((aligned(16))) float lds[8 * 128 * 2 + 8 * 64 * 2 + 64 + 8 * 16 + 32];
  float* const red = lds;
  float* const redA = lds + 1024;
  float* const redY = lds + 2048;
  unsigned* const qs = (unsigned*)(lds + 3072);
  float* const redM = lds + 3136;
  float* const sml = lds + 3264;

  const int b = blockIdx.x, h = (b & 7) * 4 + (b >> 6), s = (b >> 3) & 7;
  const int tid = threadIdx.x, lane = tid & 63, w = __builtin_amdgcn_readfirstlane(tid >> 6);
  const int r = lane >> 4, c = lane & 15;
  ST(0);
  const __amdgpu_buffer_rsrc_t rs = __builtin_amdgcn_make_buffer_rsrc((void*)ws, 0, WS_END, RSRC_W3);

  const gptr4 xg = (gptr4)x_;
  const u32x4 xa = xg[8 * w + 2 * r], xb = xg[8 * w + 2 * r + 1];
  const gptr4 wqp = (gptr4)wq_ + (size_t)(64 * w + 16 * r) * 512 + h * 16 + c;
  const size_t kvbase = ((size_t)h * SK + s * 1024 + w * 128) * 16 + lane;
  const gptr4 Kp = (gptr4)K_ + kvbase, Vp = (gptr4)V_ + kvbase;
  u32x4 wqv[16], kv[32];
#pragma unroll
  for (int i = 0; i < 16; ++i) { wqv[i] = wqp[i * 512]; kv[i] = ldkv(Kp + i * 64); }
#pragma unroll
  for (int i = 16; i < 32; ++i) kv[i] = ldkv(Kp + i * 64);
  const int chunk = 6 * h + s;
  const bool has_chunk = (s < 6) && (chunk < F / 8);
  const __amdgpu_buffer_rsrc_t rg = __builtin_amdgcn_make_buffer_rsrc((void*)wg_, 0, has_chunk ? D * F * 2 : 0, RSRC_W3);
  const __amdgpu_buffer_rsrc_t ru = __builtin_amdgcn_make_buffer_rsrc((void*)wu_, 0, has_chunk ? D * F * 2 : 0, RSRC_W3);
  const u32x4 g8 = __builtin_amdgcn_raw_buffer_load_b128(rg, tid * (F * 2) + chunk * 16, 0, 0);
  const u32x4 u8 = __builtin_amdgcn_raw_buffer_load_b128(ru, tid * (F * 2) + chunk * 16, 0, 0);
  const unsigned short xd = ((const GAS unsigned short*)x_)[tid];
  const unsigned epoch = __builtin_amdgcn_raw_buffer_load_b32(rs, WS_EPOCH, 0, AUX_SC1);
  SB();

  {
    float qa[8] = {0.f, 0.f, 0.f, 0.f, 0.f, 0.f, 0.f, 0.f};
#pragma unroll
    for (int i = 0; i < 16; ++i) {
      const unsigned xw = (i < 8) ? xa[(i & 7) >> 1] : xb[(i & 7) >> 1];
      const float xv = (i & 1) ? hi(xw) : lo(xw);
#pragma unroll
      for (int k = 0; k < 4; ++k) { qa[2 * k] = fmaf(xv, lo(wqv[i][k]), qa[2 * k]); qa[2 * k + 1] = fmaf(xv, hi(wqv[i][k]), qa[2 * k + 1]); }
    }
    float t0 = tr32(qa[0], qa[4]), t1 = tr32(qa[1], qa[5]), t2 = tr32(qa[2], qa[6]), t3 = tr32(qa[3], qa[7]);
    f32x2 u = {tr16(t0, t2), tr16(t1, t3)};
    *(f32x2*)(red + w * 128 + 2 * lane) = u;
  }
  wg_barrier();
  if (w == 0) {
    f32x2 a = *(const f32x2*)(red + 2 * lane);
#pragma unroll
    for (int j = 1; j < 8; ++j) a += *(const f32x2*)(red + j * 128 + 2 * lane);
    qs[4 * c + r] = pkbf(a[0], a[1]);
  }
  wg_barrier();
  const u32x4 qv = *(const u32x4*)(qs + 4 * c);
  ST(1);

  constexpr float C2 = 0.08838834764831845f * 1.4426950408889634f;
  float sc[32];
#pragma unroll
  for (int i = 0; i < 32; ++i) {
    float d = dot2(kv[i][0], qv[0], 0.f); d = dot2(kv[i][1], qv[1], d); d = dot2(kv[i][2], qv[2], d); d = dot2(kv[i][3], qv[3], d);
    kv[i] = ldkv(Vp + i * 64);
    d += dpp<ROR8>(d); d += dpp<ROR4>(d); d += dpp<ROR2>(d); d += dpp<ROR1>(d);
    sc[i] = d * C2;
    SB();
  }
  const int rho = lane >> 3, kap = lane & 7;
  const gptr4 wop = (gptr4)wo_ + (size_t)(h * 128 + 16 * w + 2 * rho) * 64 + s * 8 + kap;
  const u32x4 woA = wop[0], woB = wop[64];
  const __amdgpu_buffer_rsrc_t rd = __builtin_amdgcn_make_buffer_rsrc((void*)wd_, 0, w < 6 ? F * D * 2 : 0, RSRC_W3);
  const u32x4 wdv = __builtin_amdgcn_raw_buffer_load_b128(rd, ((6 * h + w) * 8 + rho) * 1024 + s * 128 + kap * 16, 0, 0);
  const u32x4 gx = xg[lane], gg = ((gptr4)gw_)[lane];
  SB();
  ST(2);

  float m = sc[0];
#pragma unroll
  for (int i = 1; i < 32; ++i) m = fmaxf(m, sc[i]);
  m = max32(max16(m));
  float l = 0.f;
#pragma unroll
  for (int i = 0; i < 32; ++i) { sc[i] = __builtin_amdgcn_exp2f(sc[i] - m); l += sc[i]; }
  l = tr16(l, l); l = tr32(l, l);
  {
    const float xv = lo(xd);
    float p[16];
#pragma unroll
    for (int k = 0; k < 4; ++k) { p[2 * k] = xv * lo(g8[k]); p[2 * k + 1] = xv * hi(g8[k]); p[8 + 2 * k] = xv * lo(u8[k]); p[9 + 2 * k] = xv * hi(u8[k]); }
#pragma unroll
    for (int k = 0; k < 8; ++k) p[k] = tr32(p[k], p[k + 8]);
#pragma unroll
    for (int k = 0; k < 4; ++k) p[k] = tr16(p[k], p[k + 4]);
#pragma unroll
    for (int k = 0; k < 2; ++k) { const float a = p[k] + dpp<ROR8>(p[k]), bb = p[k + 2] + dpp<ROR8>(p[k + 2]); p[k] = (lane & 8) ? bb : a; }
    { const float a = p[0] + dpp<XOR7>(p[0]), bb = p[1] + dpp<XOR7>(p[1]); p[0] = (lane & 4) ? bb : a; }
    p[0] += dpp<XOR2>(p[0]); p[0] += dpp<XOR1>(p[0]);
    if ((lane & 3) == 0) redM[w * 16 + (lane >> 2)] = p[0];
    if (lane == 0) *(f32x2*)(sml + 2 * w) = f32x2{m, l};
  }
  wg_barrier();
  const unsigned tag = epoch * 0x9E3779B1u + 0x7F4A7C15u;
  float fw;
  {
    const f32x2 ml = *(const f32x2*)(sml + 2 * (lane & 7));
    float M = ml[0];
    M = fmaxf(M, dpp<XOR1>(M)); M = fmaxf(M, dpp<XOR2>(M)); M = fmaxf(M, dpp<XOR7>(M));
    fw = __builtin_amdgcn_exp2f(m - M);
    if (w == 0) {
      float L = ml[1] * __builtin_amdgcn_exp2f(ml[0] - M);
      L += dpp<XOR1>(L); L += dpp<XOR2>(L); L += dpp<XOR7>(L);
      if (lane == 0) __builtin_amdgcn_raw_buffer_store_b128(u32x4{asu(M), tag, asu(L), tag}, rs, WS_ML + (h * 8 + s) * 16, 0, AUX_SC1);
    } else if (w == 1) {
      float t = redM[c];
#pragma unroll
      for (int j = 1; j < 8; ++j) t += redM[j * 16 + c];
      t = rbf(t);
      const float hv = rbf(t * dpp<ROR8>(t));
      if (lane < 8) __builtin_amdgcn_raw_buffer_store_b64(u32x2{asu(hv), tag}, rs, WS_HH + ((h * 8 + s) * 8 + lane) * 8, 0, AUX_SC1);
    }
  }
  ST(3);

  float acc[8] = {0.f, 0.f, 0.f, 0.f, 0.f, 0.f, 0.f, 0.f};
  u32x4 mlg; u32x2 hg;
  const int ml_off = WS_ML + (h * 8 + kap) * 16, hh_off = WS_HH + ((h * 8 + w) * 8 + rho) * 8;
#pragma unroll
  for (int i = 0; i < 32; ++i) {
    if (i == 24) {
      mlg = __builtin_amdgcn_raw_buffer_load_b128(rs, ml_off, 0, AUX_SC1);
      hg = __builtin_amdgcn_raw_buffer_load_b64(rs, hh_off, 0, AUX_SC1);
    }
#pragma unroll
    for (int k = 0; k < 4; ++k) { acc[2 * k] = fmaf(sc[i], lo(kv[i][k]), acc[2 * k]); acc[2 * k + 1] = fmaf(sc[i], hi(kv[i][k]), acc[2 * k + 1]); }
    PIN8(acc);
    SB();
  }
  {
    float t0 = tr32(acc[0], acc[4]), t1 = tr32(acc[1], acc[5]), t2 = tr32(acc[2], acc[6]), t3 = tr32(acc[3], acc[7]);
    f32x2 u = {tr16(t0, t2) * fw, tr16(t1, t3) * fw};
    *(f32x2*)(redA + w * 128 + 2 * lane) = u;
  }
  ST(4);
  wg_barrier();
  {
    const int jj = 8 * w + kap;
    const f32x2 a = *(const f32x2*)(redA + rho * 128 + 2 * (16 * (jj & 3) + (jj >> 2)));
    float t = tr32(a[0], a[1]);
    t = tr16(t, t); t += dpp<ROR8>(t);
    auto r2 = __builtin_amdgcn_permlane32_swap(asu(t), asu(t), false, false);
    __builtin_amdgcn_raw_buffer_store_b128(u32x4{r2[0], tag, r2[1], tag}, rs, lane < 8 ? WS_ACC + (h * 64 + jj) * 128 + s * 16 : WS_END, 0, AUX_SC1);
    SB();
  }
  ST(5);
  bool ok = true;
  if (__builtin_expect(__builtin_amdgcn_ballot_w64((((mlg[1] ^ tag) | (mlg[3] ^ tag)) | (hg[1] ^ tag)) != 0u) != 0ull, 0)) {
#pragma nounroll
    for (unsigned spins = 0;;) {
      RELOAD();
      mlg = __builtin_amdgcn_raw_buffer_load_b128(rs, ml_off, 0, AUX_SC1);
      hg = __builtin_amdgcn_raw_buffer_load_b64(rs, hh_off, 0, AUX_SC1);
      if (__builtin_amdgcn_ballot_w64((((mlg[1] ^ tag) | (mlg[3] ^ tag)) | (hg[1] ^ tag)) != 0u) == 0ull) break;
      if (++spins > SPIN_CAP) { ok = false; break; }
    }
  }
  float wt;
  {
    const float ms = asf(mlg[0]);
    float M = ms;
    M = fmaxf(M, dpp<XOR1>(M)); M = fmaxf(M, dpp<XOR2>(M)); M = fmaxf(M, dpp<XOR7>(M));
    wt = __builtin_amdgcn_exp2f(ms - M);
    float L = wt * asf(mlg[2]);
    L += dpp<XOR1>(L); L += dpp<XOR2>(L); L += dpp<XOR7>(L);
    wt *= __builtin_amdgcn_rcpf(L);
  }
  {
    const float hv = asf(hg[0]);
    float y[8];
#pragma unroll
    for (int k = 0; k < 4; ++k) { y[2 * k] = hv * lo(wdv[k]); y[2 * k + 1] = hv * hi(wdv[k]); }
    float t0 = tr32(y[0], y[4]), t1 = tr32(y[1], y[5]), t2 = tr32(y[2], y[6]), t3 = tr32(y[3], y[7]);
    float u0 = tr16(t0, t2), u1 = tr16(t1, t3);
    u0 += dpp<ROR8>(u0); u1 += dpp<ROR8>(u1);
    redY[512 + w * 64 + lane] = (lane & 8) ? u1 : u0;
  }
  float gate = 0.f;
  if (w == 0) {
    float t = 0.f;
#pragma unroll
    for (int k = 0; k < 4; ++k) { t += rbf(lo(gx[k]) * lo(gg[k])); t += rbf(hi(gx[k]) * hi(gg[k])); }
    t += dpp<XOR1>(t); t += dpp<XOR2>(t); t += dpp<XOR7>(t); t += dpp<ROR8>(t);
    t = tr16(t, t); t = tr32(t, t);
    t = rbf(t);
    gate = rbf(1.f / (1.f + __expf(-t)));
  }
  float wa[8], wb[8];
#pragma unroll
  for (int k = 0; k < 4; ++k) { wa[2 * k] = lo(woA[k]); wa[2 * k + 1] = hi(woA[k]); wb[2 * k] = lo(woB[k]); wb[2 * k + 1] = hi(woB[k]); }
  PIN8(wa); PIN8(wb);
  asm volatile("" : "+v"(wt), "+v"(gate));
  SB();
  {
    const int off = WS_ACC + (h * 64 + 8 * w) * 128 + lane * 16;
    u32x4 g;
#pragma nounroll
    for (unsigned spins = 0;;) {
      RELOAD();
      g = __builtin_amdgcn_raw_buffer_load_b128(rs, off, 0, AUX_SC1);
      if (__builtin_amdgcn_ballot_w64(((g[1] ^ tag) | (g[3] ^ tag)) != 0u) == 0ull) break;
      if (++spins > SPIN_CAP) { ok = false; break; }
    }
    ST(6);
    float o0 = wt * asf(g[0]), o1 = wt * asf(g[2]);
    o0 += dpp<XOR1>(o0); o1 += dpp<XOR1>(o1); o0 += dpp<XOR2>(o0); o1 += dpp<XOR2>(o1); o0 += dpp<XOR7>(o0); o1 += dpp<XOR7>(o1);
    const unsigned op = pkbf(o0, o1);
    o0 = lo(op); o1 = hi(op);
    float y[8];
#pragma unroll
    for (int k = 0; k < 8; ++k) y[k] = fmaf(o1, wb[k], o0 * wa[k]);
    float t0 = tr32(y[0], y[4]), t1 = tr32(y[1], y[5]), t2 = tr32(y[2], y[6]), t3 = tr32(y[3], y[7]);
    float u0 = tr16(t0, t2), u1 = tr16(t1, t3);
    u0 += dpp<ROR8>(u0); u1 += dpp<ROR8>(u1);
    float yv = (lane & 8) ? u1 : u0;
    if (!ok) yv = __builtin_nanf("");
    redY[w * 64 + lane] = yv;
  }
  wg_barrier();
  if (w >= 2) return;
  {
    float t = redY[w * 512 + lane];
#pragma unroll
    for (int j = 1; j < 8; ++j) t += redY[w * 512 + j * 64 + lane];
    const int col = 8 * kap + ((lane >> 5) << 2) + (((lane >> 4) & 1) << 1) + ((lane >> 3) & 1);
    __builtin_amdgcn_raw_buffer_store_b64(u32x2{asu(t), tag}, rs, WS_Y + (32 * s + (col >> 1)) * 1024 + (32 * w + h) * 16 + (col & 1) * 8, 0, AUX_SC1);
  }
  if (w == 1) return;
  ST(7);
  {
    const int cp = 32 * s + h;
    const int off = WS_Y + cp * 1024 + lane * 16;
    u32x4 g;
#pragma nounroll
    for (unsigned spins = 0;;) {
      RELOAD();
      g = __builtin_amdgcn_raw_buffer_load_b128(rs, off, 0, AUX_SC1);
      if (__builtin_amdgcn_ballot_w64(((g[1] ^ tag) | (g[3] ^ tag)) != 0u) == 0ull) break;
      if (++spins > SPIN_CAP) { ok = false; break; }
    }
    ST(8);
    float v0 = asf(g[0]), v1 = asf(g[2]);
    v0 += dpp<XOR1>(v0); v1 += dpp<XOR1>(v1); v0 += dpp<XOR2>(v0); v1 += dpp<XOR2>(v1);
    v0 += dpp<XOR7>(v0); v1 += dpp<XOR7>(v1); v0 += dpp<ROR8>(v0); v1 += dpp<ROR8>(v1);
    float t = tr16(v0, v1);
    t = rbf(t);
    const float coef = (lane < 32) ? gate : rbf(1.f - gate);
    t = rbf(coef * t);
    t = tr32(t, t);
    const float c0 = asf(__builtin_amdgcn_readlane(asu(t), 0)), c1 = asf(__builtin_amdgcn_readlane(asu(t), 16));
    unsigned o = pkbf(c0, c1);
    if (!ok) o = 0x7fc07fc0u;
    if (lane == 0) {
      ((GAS unsigned*)out_)[cp] = o;
      if (b == 0) __builtin_amdgcn_raw_buffer_store_b32(tag, rs, WS_EPOCH, 0, AUX_SC1);
    }
  }
  ST(9);
  while (__builtin_amdgcn_s_memrealtime() < tstart + 6000ull) __builtin_amdgcn_s_sleep(1);
}

extern "C" void kernel_launch(void* const* d_in, const int* in_sizes, int n_in, void* d_out, int out_size, void* d_ws, size_t ws_size,
                              hipStream_t stream) {
  fused_decode<<<256, 512, 0, stream>>>((const unsigned short*)d_in[0], (const u32x4*)d_in[1], (const u32x4*)d_in[2],
                                        (const unsigned short*)d_in[3], (const unsigned short*)d_in[4], (const unsigned short*)d_in[6],
                                        (const unsigned short*)d_in[7], (const unsigned short*)d_in[8], (const unsigned short*)d_in[9],
                                        (unsigned*)d_out, (unsigned char*)d_ws);
}
```

```cpp
#define TAU 3790
#define TOFF 50
#include <hip/hip_runtime.h>
#include <hip/hip_bf16.h>
#include <cstdint>
#include <utility>

#define STAMP 0
#define TAU2 TAU
#define JB 48
#define LEAD 16
#define LEND 10
#define J1 26
#define J2 38
#define JWARM 108
#define SW 128
#define SWS 1
#define TOUCH 0
#define MASKED 1
constexpr int JE = 2, J0 = 6, JT = 14, CATCH = 4, RING = J2 + 1;

template <int A, int... I, class Fn> __device__ __forceinline__ void static_for_impl(std::integer_sequence<int, I...>, Fn&& f) { (f(std::integral_constant<int, A + I>{}), ...); }
template <int A, int B, class Fn> __device__ __forceinline__ void static_for(Fn&& f) { if constexpr (B > A) static_for_impl<A>(std::make_integer_sequence<int, B - A>{}, f); }
constexpr int cmin(int a, int b) { return a < b ? a : b; }
constexpr int cmax(int a, int b) { return a > b ? a : b; }
constexpr int cons(int j) { return j < J2 ? 0 : cmin(CATCH * (j - J2 + 1), cmax(cmax(j + 1 - LEAD, 128 - LEND - 2 * (127 - j)), 0)); }
constexpr int first_slot_with(int n) { int j = 0; while (cons(j) < n) ++j; return j; }
constexpr int slot_ticks(int j) { return ((j + (j >= J1 ? 3 : 0)) * TAU + (j > JB ? (j - JB) * (TAU2 - TAU) : 0)) >> 8; }

typedef unsigned u32x4 __attribute__((ext_vector_type(4)));
typedef unsigned u32x2 __attribute__((ext_vector_type(2)));
typedef unsigned long long u64x8 __attribute__((ext_vector_type(8)));
typedef unsigned long long u64x2 __attribute__((ext_vector_type(2)));
typedef float f32x2 __attribute__((ext_vector_type(2)));
typedef __bf16 bf16x2 __attribute__((ext_vector_type(2)));
typedef __amdgpu_buffer_rsrc_t rsrc_t;
#define GAS __attribute__((address_space(1)))

constexpr int D = 512, H = 32, SK = 8192, DH = 128, F = 1376;
constexpr int AUX_NT = 2, AUX_SC1 = 16, RSRC_W3 = 0x00020000;
constexpr int WS_EPOCH = 0;
constexpr int WS_T0 = 1024;
constexpr int WS_ML = 4096;
constexpr int WS_Q = 65536;
constexpr int WS_GU = WS_Q + 256 * 1024;
constexpr int WS_ACC = WS_GU + 256 * 1024;
constexpr int WS_Y = WS_ACC + 256 * 1024;
constexpr int WS_END = WS_Y + 256 * 1024;
constexpr int WS_STAMP = 4 << 20;
constexpr unsigned SPIN_CAP = 1u << 18;
constexpr int LDS_BYTES = 96 * 1024;

__device__ __forceinline__ float lo(unsigned u) { return __builtin_bit_cast(float, u << 16); }
__device__ __forceinline__ float hi(unsigned u) { return __builtin_bit_cast(float, u & 0xffff0000u); }
__device__ __forceinline__ float asf(unsigned u) { return __builtin_bit_cast(float, u); }
__device__ __forceinline__ unsigned asu(float f) { return __builtin_bit_cast(unsigned, f); }
__device__ __forceinline__ unsigned pkbf(float a, float b) {
  bf16x2 v = {(__bf16)a, (__bf16)b};
  return __builtin_bit_cast(unsigned, v);
}
__device__ __forceinline__ float rbf(float f) { return lo(pkbf(f, f)); }
__device__ __forceinline__ float dot2(unsigned k, unsigned q, float acc) {
  return __builtin_amdgcn_fdot2_f32_bf16(__builtin_bit_cast(bf16x2, k), __builtin_bit_cast(bf16x2, q), acc, false);
}
template <int CTRL> __device__ __forceinline__ int dppi(int x) { return __builtin_amdgcn_mov_dpp(x, CTRL, 0xf, 0xf, true); }
template <int CTRL> __device__ __forceinline__ float dpp(float x) { return __builtin_bit_cast(float, dppi<CTRL>(__builtin_bit_cast(int, x))); }
constexpr int XOR1 = 0xB1, XOR2 = 0x4E, XOR7 = 0x141, ROR8 = 0x128, ROR4 = 0x124, ROR12 = 0x12C, BCAST = 0x150;
__device__ __forceinline__ float sum8(float x) { x += dpp<XOR1>(x); x += dpp<XOR2>(x); x += dpp<XOR7>(x); return x; }
__device__ __forceinline__ float max8(float x) { x = fmaxf(x, dpp<XOR1>(x)); x = fmaxf(x, dpp<XOR2>(x)); x = fmaxf(x, dpp<XOR7>(x)); return x; }
__device__ __forceinline__ float tr32(float a, float b) {
  auto r = __builtin_amdgcn_permlane32_swap(asu(a), asu(b), false, false);
  return asf(r[0]) + asf(r[1]);
}
__device__ __forceinline__ float tr16(float a, float b) {
  auto r = __builtin_amdgcn_permlane16_swap(asu(a), asu(b), false, false);
  return asf(r[0]) + asf(r[1]);
}
__device__ __forceinline__ float maxhi(float a) {
  a = fmaxf(a, dpp<ROR8>(a));
  auto r = __builtin_amdgcn_permlane16_swap(asu(a), asu(a), false, false);
  a = fmaxf(asf(r[0]), asf(r[1]));
  auto t = __builtin_amdgcn_permlane32_swap(asu(a), asu(a), false, false);
  return fmaxf(asf(t[0]), asf(t[1]));
}
__device__ __forceinline__ float sumhi(float x) { x += dpp<ROR8>(x); x = tr16(x, x); return tr32(x, x); }
__device__ __forceinline__ float fold4(float a0, float a1, float b0, float b1) { float x = tr16(tr32(a0, b0), tr32(a1, b1)); return x + dpp<ROR8>(x); }
__device__ __forceinline__ float fold8(const float (&y)[8], bool b3) {
  const float t0 = tr32(y[0], y[4]), t1 = tr32(y[1], y[5]), t2 = tr32(y[2], y[6]), t3 = tr32(y[3], y[7]);
  const float u0 = tr16(t0, t2), u1 = tr16(t1, t3);
  return (b3 ? u1 : u0) + dpp<ROR8>(b3 ? u0 : u1);
}
__device__ __forceinline__ f32x2 fold8to2(const float (&y)[8]) {
  const float t0 = tr32(y[0], y[4]), t1 = tr32(y[1], y[5]), t2 = tr32(y[2], y[6]), t3 = tr32(y[3], y[7]);
  return f32x2{tr16(t0, t2), tr16(t1, t3)};
}
__device__ __forceinline__ float bperm(int src_lane, float x) { return asf(__builtin_amdgcn_ds_bpermute(src_lane * 4, asu(x))); }
__device__ __forceinline__ void wg_barrier() {
  __builtin_amdgcn_fence(__ATOMIC_RELEASE, "workgroup");
  __builtin_amdgcn_s_barrier();
  __builtin_amdgcn_fence(__ATOMIC_ACQUIRE, "workgroup");
}
__device__ __forceinline__ rsrc_t mk(const void* p, int bytes) { return __builtin_amdgcn_make_buffer_rsrc((void*)p, 0, bytes, RSRC_W3); }
#define ld128(r, voff, soff, aux) __builtin_amdgcn_raw_buffer_load_b128((r), (voff), (soff), (aux))
__device__ __forceinline__ unsigned stale(u32x4 g, unsigned tag) { return (g[1] ^ tag) | (g[3] ^ tag); }
__device__ __forceinline__ bool any(unsigned v) { return __builtin_amdgcn_ballot_w64(v != 0u) != 0ull; }
#define SB() __builtin_amdgcn_sched_barrier(0)
#define PIN8(a) asm volatile("" : "+v"(a[0]), "+v"(a[1]), "+v"(a[2]), "+v"(a[3]), "+v"(a[4]), "+v"(a[5]), "+v"(a[6]), "+v"(a[7]))
#define RELOAD() asm volatile("" ::: "memory")
#define ST(i) do { } while (0)
#define STDUMP() do { } while (0)
#define NSTAMP 14
#define STAMP_W0_FROM 8
#define STAMP_W0_TO 9

__global__ void __launch_bounds__(256, 1)
fused_decode(const void* a_x, const void* a_k, const void* a_v, const void* a_wq, const void* a_wo, const void* a_wg,
             const void* a_wu, const void* a_wd, const void* a_gw, void* a_out, void* a_ws) {
  u64x8 ka0; u64x2 ka1; unsigned long long ka2, tentry, pc; unsigned code, ctmp;
  asm volatile(
      "s_load_dwordx16 %[a0], %[ka], 0x0\n\t"
      "s_memrealtime %[t]\n\t"
      "s_getpc_b64 %[pc]\n"
      ".Lcode_from:\n\t"
      "s_lshr_b32 %[c], %[wg], 3\n\t"
      "s_lshl_b32 %[c], %[c], 10\n\t"
      "v_lshl_add_u32 %[v], %[tid], 2, %[c]\n\t"
      "v_min_u32 %[v], .Lcode_to-.Lcode_from-4, %[v]\n\t"
      "global_load_dword %[v], %[v], %[pc]\n\t"
      "s_load_dwordx4 %[a1], %[ka], 0x40\n\t"
      "s_load_dwordx2 %[a2], %[ka], 0x50"
      : [a0] "=&s"(ka0), [a1] "=&s"(ka1), [a2] "=&s"(ka2), [t] "=&s"(tentry), [pc] "=&s"(pc), [c] "=&s"(ctmp), [v] "=&v"(code)
      : [ka] "s"(__builtin_amdgcn_kernarg_segment_ptr()), [wg] "s"(blockIdx.x), [tid] "v"(threadIdx.x)
      : "scc", "memory");
  extern __shared__ __attribute__((aligned(16))) float lds[];
  float* const redQ = lds;
  float* const redG = lds + 512;
  float* const redA = lds + 1024;
  unsigned short* const qs = (unsigned short*)(lds + 1536);
  float* const sml = lds + 1600;
  float* const redY = lds + 1664;

  const int b = blockIdx.x, h = (b & 7) * 4 + (b >> 6), s = (b >> 3) & 7;
  const int me = (h * 8 + s) * 1024;
  const int tid = threadIdx.x, lane = tid & 63, w = __builtin_amdgcn_readfirstlane(tid >> 6);
  const int r = lane >> 4, c = lane & 15, rho = lane >> 3, kap = lane & 7;
  const bool b0 = lane & 1, b1 = lane & 2, b2 = lane & 4, b3 = lane & 8;
  const int row0 = 64 * s + 16 * w;
  int wq_v = r * 4 * 8192 + c * 16, wq_s = row0 * 8192 + h * 256;
  asm volatile("s_waitcnt lgkmcnt(0)" : "+s"(ka0), "+s"(ka1), "+s"(ka2), "+s"(tentry), "+v"(wq_v), "+s"(wq_s) :: "memory");
  unsigned char* const ws = (unsigned char*)ka2;
  const size_t kvb = (size_t)(h * SK + s * 1024) * 256;
  const rsrc_t rs = mk(ws, WS_END), rx = mk((const void*)ka0[0], D * 2);
  const rsrc_t rk = mk((const char*)ka0[1] + kvb, 1024 * 256), rv = mk((const char*)ka0[2] + kvb, 1024 * 256);
  unsigned tstart = (unsigned)tentry + TOFF;

  u32x4 kv[RING];
  auto issue = [&](auto J) {
    constexpr int j = J;
    if constexpr (j % 2 == 0) { while ((int)((unsigned)__builtin_amdgcn_s_memrealtime() - (tstart + slot_ticks(j))) < 0) { } }
    if constexpr (j < 64) kv[j % RING] = ld128(rk, tid * 16, j * 4096, AUX_NT); else kv[j % RING] = ld128(rv, tid * 16, (j - 64) * 4096, AUX_NT);
    if constexpr (j == 47) ST(10);
    if constexpr (j == 87) ST(11);
    if constexpr (j == 107) ST(12);
    if constexpr (j == 127) ST(13);
    SB();
  };
  const unsigned epoch = __builtin_amdgcn_raw_buffer_load_b32(rs, WS_EPOCH, 0, AUX_SC1);
  u32x4 wqv[4];
  {
    const rsrc_t rq = mk((const void*)ka0[3], D * H * DH * 2);
#pragma unroll
    for (int j = 0; j < 4; ++j) wqv[j] = ld128(rq, wq_v, wq_s + j * 8192, AUX_NT);
  }
  const u32x2 xq = __builtin_amdgcn_raw_buffer_load_b64(rx, 8 * r, row0 * 2, 0);
  SB();
  static_for<0, JE>(issue);
  const unsigned tag = epoch * 0x9E3779B1u + 0x7F4A7C15u;
  __builtin_amdgcn_raw_buffer_store_b128(u32x4{(unsigned)tentry, tag, (unsigned)tentry, tag}, rs, (b < 8 && tid < 8) ? WS_T0 + b * 128 + lane * 16 : WS_END, 0, AUX_SC1);
  SB();
  static_for<JE, J0>(issue);
  {
    float qa[8];
#pragma unroll
    for (int j = 0; j < 4; ++j) {
      const float xv = (j & 1) ? hi(xq[j >> 1]) : lo(xq[j >> 1]);
#pragma unroll
      for (int k = 0; k < 4; ++k) {
        qa[2 * k] = j ? fmaf(xv, lo(wqv[j][k]), qa[2 * k]) : xv * lo(wqv[j][k]);
        qa[2 * k + 1] = j ? fmaf(xv, hi(wqv[j][k]), qa[2 * k + 1]) : xv * hi(wqv[j][k]);
      }
    }
    *(f32x2*)(redQ + w * 128 + 2 * lane) = fold8to2(qa);
  }
  SB();
  static_for<J0, J0 + 2>(issue);
  const int pub_lds = r * 128 + 2 * (16 * (c & 3) + 4 * w + (c >> 2)), pub_off = lane < 16 ? me + (16 * w + c) * 16 : WS_END;
  auto publish_pairs = [&](const float* red, int base, bool on) {
    const f32x2 a = *(const f32x2*)(red + pub_lds);
    float t = tr32(a[0], a[1]);
    t = tr16(t, t);
    auto r2 = __builtin_amdgcn_permlane32_swap(asu(t), asu(t), false, false);
    __builtin_amdgcn_raw_buffer_store_b128(u32x4{r2[0], tag, r2[1], tag}, rs, on ? pub_off : WS_END, base, AUX_SC1);
    SB();
  };
  wg_barrier();
  publish_pairs(redQ, WS_Q, true);
  static_for<J0 + 2, JT>(issue);
  {
    const u32x2 t0g = __builtin_amdgcn_raw_buffer_load_b64(rs, WS_T0 + kap * 128, 0, AUX_SC1);
    SB();
    static_for<JT, JT + 10>(issue);
    int dlt = t0g[1] == tag ? (int)(t0g[0] - (unsigned)tentry) : 0;
    dlt = min(dlt, dppi<XOR1>(dlt)); dlt = min(dlt, dppi<XOR2>(dlt)); dlt = min(dlt, dppi<XOR7>(dlt));
    tstart += max(min(__builtin_amdgcn_readfirstlane(dlt), 0), -300);
    SB();
  }
  static_for<JT + 10, J1>(issue);
  const int goff = (h * 8 + rho) * 1024 + w * 256 + kap * 16;
  u32x4 g0 = ld128(rs, WS_Q + goff, 0, AUX_SC1), g1 = ld128(rs, WS_Q + goff + 128, 0, AUX_SC1);
  u32x4 gv[2], uv[2];
  {
    const rsrc_t rg = mk((const void*)ka0[5], D * F * 2), ru = mk((const void*)ka0[6], D * F * 2);
    const int chunk = 6 * h + kap;
    const int go = (kap < 6 && chunk < F / 8) ? rho * (F * 2) + chunk * 16 : D * F * 2;
#pragma unroll
    for (int t = 0; t < 2; ++t) { gv[t] = ld128(rg, go, (row0 + 8 * t) * (F * 2), AUX_NT); uv[t] = ld128(ru, go, (row0 + 8 * t) * (F * 2), AUX_NT); }
  }
  const unsigned short xg0 = __builtin_amdgcn_raw_buffer_load_b16(rx, 2 * rho, row0 * 2, 0);
  const unsigned short xg1 = __builtin_amdgcn_raw_buffer_load_b16(rx, 2 * rho + 16, row0 * 2, 0);
  unsigned touch = 0;
  if (TOUCH) {
    const char* p = (const char*)ka0[2] + kvb;
    if (lane == 1) p = (const char*)ka0[4] + (h * 128 + 32 * w) * 1024 + s * 128;
    if (lane == 2) p = (const char*)ka0[7] + min((6 * h + w) * 8, F - 8) * 1024 + s * 128;
    if (lane == 3) p = (const char*)ka1[0];
    if (lane == 4) p = (const char*)ka1[1];
    if (lane < TOUCH) touch = __builtin_nontemporal_load((const GAS unsigned*)p);
  }
  SB();
  static_for<J1, J2>(issue);
  bool ok = true;
  asm volatile("" :: "v"(touch));
  asm volatile("" :: "v"(code));
  if (__builtin_expect(any(stale(g0, tag) | stale(g1, tag)), 0)) {
#pragma nounroll
    for (unsigned spins = 0;;) {
      RELOAD();
      g0 = ld128(rs, WS_Q + goff, 0, AUX_SC1); g1 = ld128(rs, WS_Q + goff + 128, 0, AUX_SC1);
      if (!any(stale(g0, tag) | stale(g1, tag))) break;
      if (++spins > SPIN_CAP) { ok = false; break; }
    }
  }
  {
    const float qd = fold4(asf(g0[0]), asf(g0[2]), asf(g1[0]), asf(g1[2]));
    qs[32 * w + (lane >> 5) * 16 + 2 * kap + (r & 1)] = (unsigned short)pkbf(qd, qd);
    float p[16];
    const float x0 = lo(xg0), x1 = lo(xg1);
#pragma unroll
    for (int k = 0; k < 4; ++k) {
      p[2 * k] = fmaf(x1, lo(gv[1][k]), x0 * lo(gv[0][k])); p[2 * k + 1] = fmaf(x1, hi(gv[1][k]), x0 * hi(gv[0][k]));
      p[8 + 2 * k] = fmaf(x1, lo(uv[1][k]), x0 * lo(uv[0][k])); p[9 + 2 * k] = fmaf(x1, hi(uv[1][k]), x0 * hi(uv[0][k]));
    }
#pragma unroll
    for (int k = 0; k < 8; ++k) p[k] = tr32(p[k], p[k + 8]);
#pragma unroll
    for (int k = 0; k < 4; ++k) p[k] = tr16(p[k], p[k + 4]);
#pragma unroll
    for (int k = 0; k < 2; ++k) p[k] = (b3 ? p[k + 2] : p[k]) + dpp<ROR8>(b3 ? p[k] : p[k + 2]);
    const int slot = 8 * kap + rho;
    *(f32x2*)(redG + w * 128 + 2 * (16 * (slot & 3) + (slot >> 2))) = f32x2{p[0], p[1]};
  }
  wg_barrier();
  const u32x4 qv = *(const u32x4*)(qs + 8 * c);
  publish_pairs(redG, WS_GU, w < 3);
  ST(1);

  constexpr float C2 = 0.08838834764831845f * 1.4426950408889634f;
  float sc[4], f1, f2, f4, f8;
  auto eat_k = [&](auto I) {
    constexpr int i = I;
    const u32x4 k = kv[i % RING];
    float d = dot2(k[0], qv[0], 0.f); d = dot2(k[1], qv[1], d); d = dot2(k[2], qv[2], d); d = dot2(k[3], qv[3], d);
    if constexpr (!(i & 1)) f1 = d;
    else {
      d = (b0 ? d : f1) + dpp<XOR1>(b0 ? f1 : d);
      if constexpr (!(i & 2)) f2 = d;
      else {
        d = (b1 ? d : f2) + dpp<XOR2>(b1 ? f2 : d);
        if constexpr (!(i & 4)) f4 = d;
        else {
          const float t1 = f4 + dpp<ROR12>(f4), t2 = d + dpp<ROR4>(d);
          d = b2 ? t2 : t1;
          if constexpr (!(i & 8)) f8 = d;
          else { d = (b3 ? d : f8) + dpp<ROR8>(b3 ? f8 : d); sc[i >> 4] = d * C2; }
        }
      }
    }
    asm volatile("" : "+v"(d));
    SB();
  };
  constexpr int JK = first_slot_with(64);
  static_assert(cons(JK) == 64 && LEAD < RING && LEND >= 6 && JK + 3 < JWARM && JWARM + LEAD < 128 && SW > JK + 3, "schedule");
  static_for<J2, JK + 1>([&](auto J) { constexpr int j = J; issue(J); static_for<cons(j - 1), cons(j)>(eat_k); });
  ST(2);
  const float m = maxhi(max8(fmaxf(fmaxf(sc[0], sc[1]), fmaxf(sc[2], sc[3]))));
#pragma unroll
  for (int i = 0; i < 4; ++i) sc[i] = __builtin_amdgcn_exp2f(sc[i] - m);
  const float l = sumhi(sum8((sc[0] + sc[1]) + (sc[2] + sc[3])));
  if (lane == 0) *(f32x2*)(sml + 2 * w) = f32x2{m, l};

  float acc[8] = {0.f, 0.f, 0.f, 0.f, 0.f, 0.f, 0.f, 0.f};
  auto eat_v = [&](auto I) {
    constexpr int i = I - 64;
    const u32x4 v = kv[(64 + i) % RING];
    const float p = dpp<BCAST + (i & 15)>(sc[i >> 4]);
#pragma unroll
    for (int k = 0; k < 4; ++k) { acc[2 * k] = fmaf(p, lo(v[k]), acc[2 * k]); acc[2 * k + 1] = fmaf(p, hi(v[k]), acc[2 * k + 1]); }
    PIN8(acc);
    SB();
  };
  u32x4 wt8[6], warm;
  const rsrc_t ro = mk((const void*)ka0[4], H * DH * D * 2), rd = mk((const void*)ka0[7], F * D * 2);
  const int dro = (2 * (rho & 3) + (rho >> 2)) * 1024 + kap * 16 + s * 128;
  auto tile = [&](auto T) {
    constexpr int t = T;
    if constexpr (t < 4) wt8[t] = ld128(ro, rho * 2048 + kap * 16, (h * 128 + 32 * w + 16 * (t >> 1) + (t & 1)) * 1024 + s * 128, AUX_NT);
    else if constexpr (t == 4) wt8[4] = ld128(rd, dro + (6 * h + w) * 8192, 0, AUX_NT);
    else wt8[5] = ld128(rd, w < 2 ? dro + (6 * h + w + 4) * 8192 : F * D * 2, 0, AUX_NT);
    SB();
  };
  auto step_v = [&](auto J) {
    constexpr int j = J;
    issue(J);
    if constexpr (j >= SW && (j - SW) % SWS == 0 && (j - SW) / SWS < 6) tile(std::integral_constant<int, 5 - (j - SW) / SWS>{});
    if constexpr (j == JWARM) {
      warm = ld128(rs, r == 0 ? WS_ACC + me + w * 256 + c * 16 : r == 1 ? WS_Y + me + w * 256 + c * 16 : WS_END, 0, AUX_SC1);
      SB();
    }
    static_for<cons(j - 1), cons(j)>(eat_v);
  };
  static_for<JK + 1, JK + 3>(step_v);
  wg_barrier();
  float fw;
  {
    const f32x2 ml = *(const f32x2*)(sml + 2 * (lane & 3));
    float M = ml[0];
    M = fmaxf(M, dpp<XOR1>(M)); M = fmaxf(M, dpp<XOR2>(M));
    fw = __builtin_amdgcn_exp2f(m - M);
    float L = ml[1] * __builtin_amdgcn_exp2f(ml[0] - M);
    L += dpp<XOR1>(L); L += dpp<XOR2>(L);
    __builtin_amdgcn_raw_buffer_store_b128(u32x4{asu(M), tag, asu(L), tag}, rs, tid < 8 ? WS_ML + (h * 8 + s) * 128 + tid * 16 : WS_END, 0, AUX_SC1);
    asm volatile("" : "+v"(fw));
    SB();
  }
  ST(3);
  static_for<JK + 3, 128>(step_v);
  asm volatile("" :: "v"(warm));
  const int gu_off = WS_GU + (h * 8 + rho) * 1024 + w * 128 + kap * 16;
  const int gu_off1 = w < 2 ? gu_off + 512 : WS_END;
  const unsigned tag1 = w < 2 ? tag : 0u;
  const int ml_off = WS_ML + (h * 8 + rho) * 128 + kap * 16;
  constexpr int NT8 = SW > 127 ? 0 : cmin(6, (127 - SW) / SWS + 1);
  static_for<0, cmax(2 - NT8, 0)>([&](auto T) { tile(std::integral_constant<int, 5 - NT8 - T>{}); });
  const u32x4 gx = ld128(rx, w == 0 ? lane * 16 : D * 2, 0, 0), gg = ld128(mk((const void*)ka1[0], D * 2), w == 0 ? lane * 16 : D * 2, 0, 0);
  u32x4 gu0 = ld128(rs, gu_off, 0, AUX_SC1), gu1 = ld128(rs, gu_off1, 0, AUX_SC1), mlg = ld128(rs, ml_off, 0, AUX_SC1);
  SB();
  static_for<0, 4>([&](auto T) { constexpr int t = T; if constexpr (t < 6 - cmax(NT8, 2)) tile(T); eat_v(std::integral_constant<int, cons(127) + t>{}); });
  static_for<cons(127) + 4, 128>(eat_v);
  *(f32x2*)(redA + w * 128 + 2 * lane) = fold8to2(acc) * fw;
  ST(4);
  wg_barrier();
  publish_pairs(redA, WS_ACC, true);
  ST(5);
  if (__builtin_expect(any(stale(gu0, tag) | stale(gu1, tag1) | stale(mlg, tag)), 0)) {
#pragma nounroll
    for (unsigned spins = 0;;) {
      RELOAD();
      gu0 = ld128(rs, gu_off, 0, AUX_SC1); gu1 = ld128(rs, gu_off1, 0, AUX_SC1); mlg = ld128(rs, ml_off, 0, AUX_SC1);
      if (!any(stale(gu0, tag) | stale(gu1, tag1) | stale(mlg, tag))) break;
      if (++spins > SPIN_CAP) { ok = false; break; }
    }
  }
  float wt;
  {
    const float ms = asf(mlg[0]);
    wt = __builtin_amdgcn_exp2f(ms - maxhi(ms));
    wt *= __builtin_amdgcn_rcpf(sumhi(wt * asf(mlg[2])));
    const float gu = rbf(fold4(asf(gu0[0]), asf(gu0[2]), asf(gu1[0]), asf(gu1[2])));
    const int src = (rho & 3) + 16 * (rho >> 2);
    const float h0 = rbf(bperm(src, gu) * bperm(src + 4, gu)), h1 = rbf(bperm(src + 32, gu) * bperm(src + 36, gu));
    float y[8];
#pragma unroll
    for (int k = 0; k < 4; ++k) { y[2 * k] = fmaf(h1, lo(wt8[5][k]), h0 * lo(wt8[4][k])); y[2 * k + 1] = fmaf(h1, hi(wt8[5][k]), h0 * hi(wt8[4][k])); }
    redY[256 + w * 64 + lane] = fold8(y, b3);
  }
  float wf[4][8];
#pragma unroll
  for (int t = 0; t < 4; ++t) {
#pragma unroll
    for (int k = 0; k < 4; ++k) { wf[t][2 * k] = lo(wt8[t][k]); wf[t][2 * k + 1] = hi(wt8[t][k]); }
    PIN8(wf[t]);
  }
  asm volatile("" : "+v"(wt));
  SB();
  g0 = ld128(rs, WS_ACC + goff, 0, AUX_SC1); g1 = ld128(rs, WS_ACC + goff + 128, 0, AUX_SC1);
  if (__builtin_expect(any(stale(g0, tag) | stale(g1, tag)), 0)) {
#pragma nounroll
    for (unsigned spins = 0;;) {
      RELOAD();
      const bool s0 = !MASKED || stale(g0, tag) != 0u, s1 = !MASKED || stale(g1, tag) != 0u;
      const u32x4 n0 = ld128(rs, s0 ? WS_ACC + goff : WS_END, 0, AUX_SC1), n1 = ld128(rs, s1 ? WS_ACC + goff + 128 : WS_END, 0, AUX_SC1);
      if (s0) g0 = n0;
      if (s1) g1 = n1;
      if (!any(stale(g0, tag) | stale(g1, tag))) break;
      if (++spins > SPIN_CAP) { ok = false; break; }
    }
  }
  ST(6);
  {
    const float od = rbf(fold4(wt * asf(g0[0]), wt * asf(g0[2]), wt * asf(g1[0]), wt * asf(g1[2])));
    const float o0 = bperm(rho, od), o1 = bperm(rho + 16, od), o2 = bperm(rho + 32, od), o3 = bperm(rho + 48, od);
    float y[8];
#pragma unroll
    for (int k = 0; k < 8; ++k) y[k] = fmaf(o3, wf[3][k], fmaf(o2, wf[2][k], fmaf(o1, wf[1][k], o0 * wf[0][k])));
    const float yv = fold8(y, b3);
    redY[w * 64 + lane] = ok ? yv : __builtin_nanf("");
  }
  wg_barrier();
  if (w >= 2) { ST(7); STDUMP(); return; }
  {
    const float* p = redY + w * 256 + 8 * kap + rho;
    const float t = (p[0] + p[64]) + (p[128] + p[192]);
    __builtin_amdgcn_raw_buffer_store_b64(u32x2{asu(t), tag}, rs, WS_Y + me + w * 512 + lane * 8, 0, AUX_SC1);
  }
  ST(7);
  if (w == 1) { STDUMP(); return; }
  float gate = 0.f;
#pragma unroll
  for (int k = 0; k < 4; ++k) { gate += rbf(lo(gx[k]) * lo(gg[k])); gate += rbf(hi(gx[k]) * hi(gg[k])); }
  gate = rbf(sumhi(sum8(gate)));
  gate = rbf(1.f / (1.f + __expf(-gate)));
  if (lane >= 32) gate = rbf(1.f - gate);
  asm volatile("" : "+v"(gate));
  SB();
  const int y_off = WS_Y + ((lane & 31) * 8 + s) * 1024 + (lane >> 5) * 512 + h * 16;
  g0 = ld128(rs, y_off, 0, AUX_SC1);
#pragma nounroll
  for (unsigned spins = 0; any(stale(g0, tag));) {
    RELOAD();
    const bool s0 = !MASKED || stale(g0, tag) != 0u;
    const u32x4 n0 = ld128(rs, s0 ? y_off : WS_END, 0, AUX_SC1);
    if (s0) g0 = n0;
    if (++spins > SPIN_CAP) { ok = false; break; }
  }
  ST(8);
  {
    float v0 = sum8(asf(g0[0])), v1 = sum8(asf(g0[2]));
    v0 += dpp<ROR8>(v0); v1 += dpp<ROR8>(v1);
    float t = rbf(tr16(v0, v1));
    t = rbf(gate * t);
    t = tr32(t, t);
    auto r2 = __builtin_amdgcn_permlane16_swap(asu(t), asu(t), false, false);
    const unsigned o = ok ? pkbf(asf(r2[0]), asf(r2[1])) : 0x7fc07fc0u;
    __builtin_amdgcn_raw_buffer_store_b32(o, mk((const void*)ka1[1], D * 2), lane == 0 ? (32 * s + h) * 4 : D * 2, 0, 0);
    if (b == 0) __builtin_amdgcn_raw_buffer_store_b32(tag, rs, lane == 0 ? WS_EPOCH : WS_END, 0, AUX_SC1);
  }
  ST(9);
  STDUMP();
  asm volatile(".Lcode_to:");
}

extern "C" void kernel_launch(void* const* d_in, const int* in_sizes, int n_in, void* d_out, int out_size, void* d_ws, size_t ws_size,
                              hipStream_t stream) {
  (void)hipFuncSetAttribute((const void*)fused_decode, hipFuncAttributeMaxDynamicSharedMemorySize, LDS_BYTES);
  fused_decode<<<256, 256, LDS_BYTES, stream>>>(d_in[0], d_in[1], d_in[2], d_in[3], d_in[4], d_in[6], d_in[7], d_in[8], d_in[9], d_out, d_ws);
}
```

```cpp
#define TAU 3686
#define TOFF 60
#include <hip/hip_runtime.h>
#include <hip/hip_bf16.h>
#include <cstdint>
#include <utility>

#define STAMP 0
#define TAU2 TAU
#define JB 48
#define LEAD 16
#define LEND 10
#define J1 32
#define J2 44
#define JWARM 108
#define SW 128
#define SWS 1
#define TOUCH 0
#define EPOCH_S 1
#define HINT_AUX 0
#define CBASE 1
#define S10 47
#define S11 87
#define S12 107
#define S13 127
#define J0 16
#define JT 10
#define JU 22
#define JC 38
#define WARMQ 0
#define QG_AUX AUX_SC1
#define WARMG 0
#define MASKED 1
constexpr int JE = 2, CATCH = 4, RING = J2 + 1;

template <int A, int... I, class Fn> __device__ __forceinline__ void static_for_impl(std::integer_sequence<int, I...>, Fn&& f) { (f(std::integral_constant<int, A + I>{}), ...); }
template <int A, int B, class Fn> __device__ __forceinline__ void static_for(Fn&& f) { if constexpr (B > A) static_for_impl<A>(std::make_integer_sequence<int, B - A>{}, f); }
constexpr int cmin(int a, int b) { return a < b ? a : b; }
constexpr int cmax(int a, int b) { return a > b ? a : b; }
constexpr int cons(int j) { return j < J2 ? 0 : cmin(CATCH * (j - J2 + 1), cmax(cmax(j + 1 - LEAD, 128 - LEND - 2 * (127 - j)), 0)); }
constexpr int first_slot_with(int n) { int j = 0; while (cons(j) < n) ++j; return j; }
constexpr int slot_ticks(int j) { return ((j + (j >= JU ? 3 : 0)) * TAU + (j > JB ? (j - JB) * (TAU2 - TAU) : 0)) >> 8; }

typedef unsigned u32x4 __attribute__((ext_vector_type(4)));
typedef unsigned u32x2 __attribute__((ext_vector_type(2)));
typedef unsigned long long u64x8 __attribute__((ext_vector_type(8)));
typedef unsigned long long u64x2 __attribute__((ext_vector_type(2)));
typedef float f32x2 __attribute__((ext_vector_type(2)));
typedef __bf16 bf16x2 __attribute__((ext_vector_type(2)));
typedef __amdgpu_buffer_rsrc_t rsrc_t;
#define GAS __attribute__((address_space(1)))

constexpr int D = 512, H = 32, SK = 8192, DH = 128, F = 1376;
constexpr int AUX_NT = 2, AUX_SC1 = 16, RSRC_W3 = 0x00020000;
constexpr int WS_EPOCH = 0;
constexpr int WS_T0 = 1024;
constexpr int WS_ML = 4096;
constexpr int WS_Q = 65536;
constexpr int WS_GU = WS_Q + 256 * 1024;
constexpr int WS_ACC = WS_GU + 256 * 1024;
constexpr int WS_Y = WS_ACC + 256 * 1024;
constexpr int WS_END = WS_Y + 256 * 1024;
constexpr int WS_STAMP = 4 << 20;
constexpr unsigned SPIN_CAP = 1u << 18;
constexpr int LDS_BYTES = 96 * 1024;

__device__ __forceinline__ float lo(unsigned u) { return __builtin_bit_cast(float, u << 16); }
__device__ __forceinline__ float hi(unsigned u) { return __builtin_bit_cast(float, u & 0xffff0000u); }
__device__ __forceinline__ float asf(unsigned u) { return __builtin_bit_cast(float, u); }
__device__ __forceinline__ unsigned asu(float f) { return __builtin_bit_cast(unsigned, f); }
__device__ __forceinline__ unsigned pkbf(float a, float b) {
  bf16x2 v = {(__bf16)a, (__bf16)b};
  return __builtin_bit_cast(unsigned, v);
}
__device__ __forceinline__ float rbf(float f) { return lo(pkbf(f, f)); }
__device__ __forceinline__ float dot2(unsigned k, unsigned q, float acc) {
  return __builtin_amdgcn_fdot2_f32_bf16(__builtin_bit_cast(bf16x2, k), __builtin_bit_cast(bf16x2, q), acc, false);
}
template <int CTRL> __device__ __forceinline__ int dppi(int x) { return __builtin_amdgcn_mov_dpp(x, CTRL, 0xf, 0xf, true); }
template <int CTRL> __device__ __forceinline__ float dpp(float x) { return __builtin_bit_cast(float, dppi<CTRL>(__builtin_bit_cast(int, x))); }
constexpr int XOR1 = 0xB1, XOR2 = 0x4E, XOR7 = 0x141, ROR8 = 0x128, ROR4 = 0x124, ROR12 = 0x12C, BCAST = 0x150;
__device__ __forceinline__ float sum8(float x) { x += dpp<XOR1>(x); x += dpp<XOR2>(x); x += dpp<XOR7>(x); return x; }
__device__ __forceinline__ float max8(float x) { x = fmaxf(x, dpp<XOR1>(x)); x = fmaxf(x, dpp<XOR2>(x)); x = fmaxf(x, dpp<XOR7>(x)); return x; }
__device__ __forceinline__ float tr32(float a, float b) {
  auto r = __builtin_amdgcn_permlane32_swap(asu(a), asu(b), false, false);
  return asf(r[0]) + asf(r[1]);
}
__device__ __forceinline__ float tr16(float a, float b) {
  auto r = __builtin_amdgcn_permlane16_swap(asu(a), asu(b), false, false);
  return asf(r[0]) + asf(r[1]);
}
__device__ __forceinline__ float maxhi(float a) {
  a = fmaxf(a, dpp<ROR8>(a));
  auto r = __builtin_amdgcn_permlane16_swap(asu(a), asu(a), false, false);
  a = fmaxf(asf(r[0]), asf(r[1]));
  auto t = __builtin_amdgcn_permlane32_swap(asu(a), asu(a), false, false);
  return fmaxf(asf(t[0]), asf(t[1]));
}
__device__ __forceinline__ float sumhi(float x) { x += dpp<ROR8>(x); x = tr16(x, x); return tr32(x, x); }
__device__ __forceinline__ float fold4(float a0, float a1, float b0, float b1) { float x = tr16(tr32(a0, b0), tr32(a1, b1)); return x + dpp<ROR8>(x); }
__device__ __forceinline__ float fold8(const float (&y)[8], bool b3) {
  const float t0 = tr32(y[0], y[4]), t1 = tr32(y[1], y[5]), t2 = tr32(y[2], y[6]), t3 = tr32(y[3], y[7]);
  const float u0 = tr16(t0, t2), u1 = tr16(t1, t3);
  return (b3 ? u1 : u0) + dpp<ROR8>(b3 ? u0 : u1);
}
__device__ __forceinline__ f32x2 fold8to2(const float (&y)[8]) {
  const float t0 = tr32(y[0], y[4]), t1 = tr32(y[1], y[5]), t2 = tr32(y[2], y[6]), t3 = tr32(y[3], y[7]);
  return f32x2{tr16(t0, t2), tr16(t1, t3)};
}
__device__ __forceinline__ float bperm(int src_lane, float x) { return asf(__builtin_amdgcn_ds_bpermute(src_lane * 4, asu(x))); }
__device__ __forceinline__ void wg_barrier() {
  __builtin_amdgcn_fence(__ATOMIC_RELEASE, "workgroup");
  __builtin_amdgcn_s_barrier();
  __builtin_amdgcn_fence(__ATOMIC_ACQUIRE, "workgroup");
}
__device__ __forceinline__ rsrc_t mk(const void* p, int bytes) { return __builtin_amdgcn_make_buffer_rsrc((void*)p, 0, bytes, RSRC_W3); }
#define ld128(r, voff, soff, aux) __builtin_amdgcn_raw_buffer_load_b128((r), (voff), (soff), (aux))
__device__ __forceinline__ unsigned stale(u32x4 g, unsigned tag) { return (g[1] ^ tag) | (g[3] ^ tag); }
__device__ __forceinline__ bool any(unsigned v) { return __builtin_amdgcn_ballot_w64(v != 0u) != 0ull; }
#define SB() __builtin_amdgcn_sched_barrier(0)
#define PIN8(a) asm volatile("" : "+v"(a[0]), "+v"(a[1]), "+v"(a[2]), "+v"(a[3]), "+v"(a[4]), "+v"(a[5]), "+v"(a[6]), "+v"(a[7]))
#define RELOAD() asm volatile("" ::: "memory")
#define ST(i) do { } while (0)
#define STDUMP() do { } while (0)
#define NSTAMP 14
#define STAMP_W0_FROM 8
#define STAMP_W0_TO 9

__global__ void __launch_bounds__(256, 1)
fused_decode(const void* a_x, const void* a_k, const void* a_v, const void* a_wq, const void* a_wo, const void* a_wg,
             const void* a_wu, const void* a_wd, const void* a_gw, void* a_out, void* a_ws) {
  u64x8 ka0; u64x2 ka1; unsigned long long ka2, tentry, pc; unsigned code, ctmp;
  asm volatile(
      "s_load_dwordx16 %[a0], %[ka], 0x0\n\t"
      "s_memrealtime %[t]\n\t"
      "s_getpc_b64 %[pc]\n"
      ".Lcode_from:\n\t"
      "s_lshr_b32 %[c], %[wg], 3\n\t"
      "s_lshl_b32 %[c], %[c], 10\n\t"
      "v_lshl_add_u32 %[v], %[tid], 2, %[c]\n\t"
      "v_min_u32 %[v], .Lcode_to-.Lcode_from-4, %[v]\n\t"
      "global_load_dword %[v], %[v], %[pc]\n\t"
      "s_load_dwordx4 %[a1], %[ka], 0x40\n\t"
      "s_load_dwordx2 %[a2], %[ka], 0x50"
      : [a0] "=&s"(ka0), [a1] "=&s"(ka1), [a2] "=&s"(ka2), [t] "=&s"(tentry), [pc] "=&s"(pc), [c] "=&s"(ctmp), [v] "=&v"(code)
      : [ka] "s"(__builtin_amdgcn_kernarg_segment_ptr()), [wg] "s"(blockIdx.x), [tid] "v"(threadIdx.x)
      : "scc", "memory");
  extern __shared__ __attribute__((aligned(16))) float lds[];
  float* const redQ = lds;
  float* const redG = lds + 512;
  float* const redA = lds + 1024;
  unsigned short* const qs = (unsigned short*)(lds + 1536);
  float* const sml = lds + 1600;
  float* const redY = lds + 1664;

  const int b = blockIdx.x, h = (b & 7) * 4 + (b >> 6), s = (b >> 3) & 7;
  const int me = (h * 8 + s) * 1024;
  const int tid = threadIdx.x, lane = tid & 63, w = __builtin_amdgcn_readfirstlane(tid >> 6);
  const int r = lane >> 4, c = lane & 15, rho = lane >> 3, kap = lane & 7;
  const bool b0 = lane & 1, b1 = lane & 2, b2 = lane & 4, b3 = lane & 8;
  const int row0 = 64 * s + 16 * w;
  int wq_v = r * 4 * 8192 + c * 16, wq_s = row0 * 8192 + h * 256;
  asm volatile("s_waitcnt lgkmcnt(0)" : "+s"(ka0), "+s"(ka1), "+s"(ka2), "+s"(tentry), "+v"(wq_v), "+s"(wq_s) :: "memory");
  unsigned char* const ws = (unsigned char*)ka2;
  const size_t kvb = (size_t)(h * SK + s * 1024) * 256;
  const rsrc_t rs = mk(ws, WS_END), rx = mk((const void*)ka0[0], D * 2);
  const rsrc_t rk = mk((const char*)ka0[1] + kvb, 1024 * 256), rv = mk((const char*)ka0[2] + kvb, 1024 * 256);
  unsigned tstart = (unsigned)tentry + TOFF;

  u32x4 kv[RING];
  auto issue = [&](auto J) {
    constexpr int j = J;
    if constexpr (j % 2 == 0) { while ((int)((unsigned)__builtin_amdgcn_s_memrealtime() - (tstart + slot_ticks(j))) < 0) { } }
    if constexpr (j < 64) kv[j % RING] = ld128(rk, tid * 16, j * 4096, AUX_NT); else kv[j % RING] = ld128(rv, tid * 16, (j - 64) * 4096, AUX_NT);
    if constexpr (j == S10) ST(10);
    if constexpr (j == S11) ST(11);
    if constexpr (j == S12) ST(12);
    if constexpr (j == S13) ST(13);
    SB();
  };
  const unsigned epoch = EPOCH_S ? *(const __attribute__((address_space(4))) unsigned*)(ws + WS_EPOCH) : __builtin_amdgcn_raw_buffer_load_b32(rs, WS_EPOCH, 0, AUX_SC1);
  u32x4 wqv[4];
  {
    const rsrc_t rq = mk((const void*)ka0[3], D * H * DH * 2);
#pragma unroll
    for (int j = 0; j < 4; ++j) wqv[j] = ld128(rq, wq_v, wq_s + j * 8192, AUX_NT);
  }
  const u32x2 xq = __builtin_amdgcn_raw_buffer_load_b64(rx, 8 * r, row0 * 2, 0);
  SB();
  static_for<0, JE>(issue);
  const unsigned tag = epoch * 0x9E3779B1u + 0x7F4A7C15u;
  __builtin_amdgcn_raw_buffer_store_b128(u32x4{(unsigned)tentry, tag, (unsigned)tentry, tag}, rs, (b < 8 && tid < 8) ? WS_T0 + b * 128 + lane * 16 : WS_END, 0, AUX_SC1);
  u32x4 warmq;
  if (WARMQ) warmq = ld128(rs, r == 0 ? WS_Q + me + w * 256 + c * 16 : (r == 1 && WARMQ > 1) ? WS_GU + me + w * 256 + c * 16 : (r == 2 && WARMQ > 1 && tid < 40) ? WS_ML + (h * 8 + s) * 128 + (c & 7) * 16 : WS_END, 0, AUX_SC1);
  SB();
  static_for<JE, JT>(issue);
  const u32x2 t0g = __builtin_amdgcn_raw_buffer_load_b64(rs, WS_T0 + kap * 128, 0, HINT_AUX);
  SB();
  static_for<JT, J0>(issue);
  {
    float qa[8];
#pragma unroll
    for (int j = 0; j < 4; ++j) {
      const float xv = (j & 1) ? hi(xq[j >> 1]) : lo(xq[j >> 1]);
#pragma unroll
      for (int k = 0; k < 4; ++k) {
        qa[2 * k] = j ? fmaf(xv, lo(wqv[j][k]), qa[2 * k]) : xv * lo(wqv[j][k]);
        qa[2 * k + 1] = j ? fmaf(xv, hi(wqv[j][k]), qa[2 * k + 1]) : xv * hi(wqv[j][k]);
      }
    }
    *(f32x2*)(redQ + w * 128 + 2 * lane) = fold8to2(qa);
  }
  SB();
  static_for<J0, J0 + 2>(issue);
  const int pub_lds = r * 128 + 2 * (16 * (c & 3) + 4 * w + (c >> 2)), pub_off = lane < 16 ? me + (16 * w + c) * 16 : WS_END;
  auto publish_pairs = [&](const float* red, int base, bool on) {
    const f32x2 a = *(const f32x2*)(red + pub_lds);
    float t = tr32(a[0], a[1]);
    t = tr16(t, t);
    auto r2 = __builtin_amdgcn_permlane32_swap(asu(t), asu(t), false, false);
    __builtin_amdgcn_raw_buffer_store_b128(u32x4{r2[0], tag, r2[1], tag}, rs, on ? pub_off : WS_END, base, AUX_SC1);
    SB();
  };
  wg_barrier();
  publish_pairs(redQ, WS_Q, true);
  static_for<J0 + 2, JT + 10>(issue);
  {
    int dlt = t0g[1] == tag ? (int)(t0g[0] - (unsigned)tentry) : 0;
    dlt = min(dlt, dppi<XOR1>(dlt)); dlt = min(dlt, dppi<XOR2>(dlt)); dlt = min(dlt, dppi<XOR7>(dlt));
    tstart += max(min(__builtin_amdgcn_readfirstlane(dlt), 0), -300);
    SB();
  }
  static_for<JT + 10, JU>(issue);
  u32x4 gv[2], uv[2];
  {
    const rsrc_t rg = mk((const void*)ka0[5], D * F * 2), ru = mk((const void*)ka0[6], D * F * 2);
    const int chunk = 6 * h + kap;
    const int go = (kap < 6 && chunk < F / 8) ? rho * (F * 2) + chunk * 16 : D * F * 2;
#pragma unroll
    for (int t = 0; t < 2; ++t) { gv[t] = ld128(rg, go, (row0 + 8 * t) * (F * 2), AUX_NT); uv[t] = ld128(ru, go, (row0 + 8 * t) * (F * 2), AUX_NT); }
  }
  const unsigned short xg0 = __builtin_amdgcn_raw_buffer_load_b16(rx, 2 * rho, row0 * 2, 0);
  const unsigned short xg1 = __builtin_amdgcn_raw_buffer_load_b16(rx, 2 * rho + 16, row0 * 2, 0);
  SB();
  static_for<JU, J1>(issue);
  const int goff = (h * 8 + rho) * 1024 + w * 256 + kap * 16;
  u32x4 g0 = ld128(rs, WS_Q + goff, 0, QG_AUX), g1 = ld128(rs, WS_Q + goff + 128, 0, QG_AUX);
  u32x4 warmg;
  if (WARMG) warmg = ld128(rs, (r == 0 && w < 3) ? WS_GU + me + w * 256 + c * 16 : (tid >= 16 && tid < 24) ? WS_ML + (h * 8 + s) * 128 + (c & 7) * 16 : WS_END, 0, AUX_SC1);
  SB();
  static_for<J1, JC>(issue);
  {
    float p[16];
    const float x0 = lo(xg0), x1 = lo(xg1);
#pragma unroll
    for (int k = 0; k < 4; ++k) {
      p[2 * k] = fmaf(x1, lo(gv[1][k]), x0 * lo(gv[0][k])); p[2 * k + 1] = fmaf(x1, hi(gv[1][k]), x0 * hi(gv[0][k]));
      p[8 + 2 * k] = fmaf(x1, lo(uv[1][k]), x0 * lo(uv[0][k])); p[9 + 2 * k] = fmaf(x1, hi(uv[1][k]), x0 * hi(uv[0][k]));
    }
#pragma unroll
    for (int k = 0; k < 8; ++k) p[k] = tr32(p[k], p[k + 8]);
#pragma unroll
    for (int k = 0; k < 4; ++k) p[k] = tr16(p[k], p[k + 4]);
#pragma unroll
    for (int k = 0; k < 2; ++k) p[k] = (b3 ? p[k + 2] : p[k]) + dpp<ROR8>(b3 ? p[k] : p[k + 2]);
    const int slot = 8 * kap + rho;
    *(f32x2*)(redG + w * 128 + 2 * (16 * (slot & 3) + (slot >> 2))) = f32x2{p[0], p[1]};
    SB();
  }
  static_for<JC, J2>(issue);
  bool ok = true;
  if (WARMG) asm volatile("" :: "v"(warmg));
  if (WARMQ) asm volatile("" :: "v"(warmq));
  asm volatile("" :: "v"(code));
  if (__builtin_expect(any(stale(g0, tag) | stale(g1, tag)), 0)) {
#pragma nounroll
    for (unsigned spins = 0;;) {
      RELOAD();
      g0 = ld128(rs, WS_Q + goff, 0, AUX_SC1); g1 = ld128(rs, WS_Q + goff + 128, 0, AUX_SC1);
      if (!any(stale(g0, tag) | stale(g1, tag))) break;
      if (++spins > SPIN_CAP) { ok = false; break; }
    }
  }
  {
    const float qd = fold4(asf(g0[0]), asf(g0[2]), asf(g1[0]), asf(g1[2]));
    qs[32 * w + (lane >> 5) * 16 + 2 * kap + (r & 1)] = (unsigned short)pkbf(qd, qd);
  }
  wg_barrier();
  const u32x4 qv = *(const u32x4*)(qs + 8 * c);
  publish_pairs(redG, WS_GU, w < 3);
  ST(1);

  constexpr float C2 = 0.08838834764831845f * 1.4426950408889634f;
  float sc[4], f1, f2, f4, f8;
  auto eat_k = [&](auto I) {
    constexpr int i = I;
    const u32x4 k = kv[i % RING];
    float d = dot2(k[0], qv[0], 0.f); d = dot2(k[1], qv[1], d); d = dot2(k[2], qv[2], d); d = dot2(k[3], qv[3], d);
    if constexpr (!(i & 1)) f1 = d;
    else {
      d = (b0 ? d : f1) + dpp<XOR1>(b0 ? f1 : d);
      if constexpr (!(i & 2)) f2 = d;
      else {
        d = (b1 ? d : f2) + dpp<XOR2>(b1 ? f2 : d);
        if constexpr (!(i & 4)) f4 = d;
        else {
          const float t1 = f4 + dpp<ROR12>(f4), t2 = d + dpp<ROR4>(d);
          d = b2 ? t2 : t1;
          if constexpr (!(i & 8)) f8 = d;
          else { d = (b3 ? d : f8) + dpp<ROR8>(b3 ? f8 : d); sc[i >> 4] = d * C2; }
        }
      }
    }
    asm volatile("" : "+v"(d));
    SB();
  };
  constexpr int JK = first_slot_with(64);
  static_assert(JE < JT && JT <= J0 && J0 + 2 <= JT + 10 && JT + 10 <= JU && JU <= J1 && J1 <= JC && JC <= J2 && cons(JK) == 64 && LEAD < RING && LEND >= 6 && JK + 3 < JWARM && JWARM + LEAD < 128 && SW > JK + 3, "schedule");
  static_for<J2, JK + 1>([&](auto J) { constexpr int j = J; issue(J); static_for<cons(j - 1), cons(j)>(eat_k); });
  ST(2);
  const float m = maxhi(max8(fmaxf(fmaxf(sc[0], sc[1]), fmaxf(sc[2], sc[3]))));
#pragma unroll
  for (int i = 0; i < 4; ++i) sc[i] = __builtin_amdgcn_exp2f(sc[i] - m);
  const float l = sumhi(sum8((sc[0] + sc[1]) + (sc[2] + sc[3])));
  if (lane == 0) *(f32x2*)(sml + 2 * w) = f32x2{m, l};

  float acc[8] = {0.f, 0.f, 0.f, 0.f, 0.f, 0.f, 0.f, 0.f};
  auto eat_v = [&](auto I) {
    constexpr int i = I - 64;
    const u32x4 v = kv[(64 + i) % RING];
    const float p = dpp<BCAST + (i & 15)>(sc[i >> 4]);
#pragma unroll
    for (int k = 0; k < 4; ++k) { acc[2 * k] = fmaf(p, lo(v[k]), acc[2 * k]); acc[2 * k + 1] = fmaf(p, hi(v[k]), acc[2 * k + 1]); }
    PIN8(acc);
    SB();
  };
  u32x4 wt8[6], warm;
  const rsrc_t ro = mk((const void*)ka0[4], H * DH * D * 2), rd = mk((const void*)ka0[7], F * D * 2);
  const int dro = (2 * (rho & 3) + (rho >> 2)) * 1024 + kap * 16 + s * 128;
  auto tile = [&](auto T) {
    constexpr int t = T;
    if constexpr (t < 4) wt8[t] = ld128(ro, rho * 2048 + kap * 16, (h * 128 + 32 * w + 16 * (t >> 1) + (t & 1)) * 1024 + s * 128, AUX_NT);
    else if constexpr (t == 4) wt8[4] = ld128(rd, dro + (6 * h + w) * 8192, 0, AUX_NT);
    else wt8[5] = ld128(rd, w < 2 ? dro + (6 * h + w + 4) * 8192 : F * D * 2, 0, AUX_NT);
    SB();
  };
  auto step_v = [&](auto J) {
    constexpr int j = J;
    issue(J);
    if constexpr (j >= SW && (j - SW) % SWS == 0 && (j - SW) / SWS < 6) tile(std::integral_constant<int, 5 - (j - SW) / SWS>{});
    if constexpr (j == JWARM) {
      warm = ld128(rs, r == 0 ? WS_ACC + me + w * 256 + c * 16 : r == 1 ? WS_Y + me + w * 256 + c * 16 : WS_END, 0, AUX_SC1);
      SB();
    }
    static_for<cons(j - 1), cons(j)>(eat_v);
  };
  static_for<JK + 1, JK + 3>(step_v);
  wg_barrier();
  float fw;
  {
    const f32x2 ml = *(const f32x2*)(sml + 2 * (lane & 3));
    float M = ml[0];
    M = fmaxf(M, dpp<XOR1>(M)); M = fmaxf(M, dpp<XOR2>(M));
    fw = __builtin_amdgcn_exp2f(m - M);
    float L = ml[1] * __builtin_amdgcn_exp2f(ml[0] - M);
    L += dpp<XOR1>(L); L += dpp<XOR2>(L);
    __builtin_amdgcn_raw_buffer_store_b128(u32x4{asu(M), tag, asu(L), tag}, rs, tid < 8 ? WS_ML + (h * 8 + s) * 128 + tid * 16 : WS_END, 0, AUX_SC1);
    asm volatile("" : "+v"(fw));
    SB();
  }
  ST(3);
  static_for<JK + 3, 128>(step_v);
  asm volatile("" :: "v"(warm));
  const int gu_off = WS_GU + (h * 8 + rho) * 1024 + w * 128 + kap * 16;
  const int gu_off1 = w < 2 ? gu_off + 512 : WS_END;
  const unsigned tag1 = w < 2 ? tag : 0u;
  const int ml_off = WS_ML + (h * 8 + rho) * 128 + kap * 16;
  constexpr int NT8 = SW > 127 ? 0 : cmin(6, (127 - SW) / SWS + 1);
  static_for<0, cmax(2 - NT8, 0)>([&](auto T) { tile(std::integral_constant<int, 5 - NT8 - T>{}); });
  const u32x4 gx = ld128(rx, w == 0 ? lane * 16 : D * 2, 0, 0), gg = ld128(mk((const void*)ka1[0], D * 2), w == 0 ? lane * 16 : D * 2, 0, 0);
  u32x4 gu0 = ld128(rs, gu_off, 0, AUX_SC1), gu1 = ld128(rs, gu_off1, 0, AUX_SC1), mlg = ld128(rs, ml_off, 0, AUX_SC1);
  SB();
  static_for<0, 4>([&](auto T) { constexpr int t = T; if constexpr (t < 6 - cmax(NT8, 2)) tile(T); eat_v(std::integral_constant<int, cons(127) + t>{}); });
  static_for<cons(127) + 4, 128>(eat_v);
  *(f32x2*)(redA + w * 128 + 2 * lane) = fold8to2(acc) * fw;
  ST(4);
  wg_barrier();
  publish_pairs(redA, WS_ACC, true);
  ST(5);
  if (__builtin_expect(any(stale(gu0, tag) | stale(gu1, tag1) | stale(mlg, tag)), 0)) {
#pragma nounroll
    for (unsigned spins = 0;;) {
      RELOAD();
      gu0 = ld128(rs, gu_off, 0, AUX_SC1); gu1 = ld128(rs, gu_off1, 0, AUX_SC1); mlg = ld128(rs, ml_off, 0, AUX_SC1);
      if (!any(stale(gu0, tag) | stale(gu1, tag1) | stale(mlg, tag))) break;
      if (++spins > SPIN_CAP) { ok = false; break; }
    }
  }
  float wt;
  {
    const float ms = asf(mlg[0]);
    wt = __builtin_amdgcn_exp2f(ms - maxhi(ms));
    wt *= __builtin_amdgcn_rcpf(sumhi(wt * asf(mlg[2])));
    const float gu = rbf(fold4(asf(gu0[0]), asf(gu0[2]), asf(gu1[0]), asf(gu1[2])));
    const int src = (rho & 3) + 16 * (rho >> 2);
    const float h0 = rbf(bperm(src, gu) * bperm(src + 4, gu)), h1 = rbf(bperm(src + 32, gu) * bperm(src + 36, gu));
    float y[8];
#pragma unroll
    for (int k = 0; k < 4; ++k) { y[2 * k] = fmaf(h1, lo(wt8[5][k]), h0 * lo(wt8[4][k])); y[2 * k + 1] = fmaf(h1, hi(wt8[5][k]), h0 * hi(wt8[4][k])); }
    redY[256 + w * 64 + lane] = fold8(y, b3);
  }
  float wf[4][8];
#pragma unroll
  for (int t = 0; t < 4; ++t) {
#pragma unroll
    for (int k = 0; k < 4; ++k) { wf[t][2 * k] = lo(wt8[t][k]); wf[t][2 * k + 1] = hi(wt8[t][k]); }
    PIN8(wf[t]);
  }
  asm volatile("" : "+v"(wt));
  SB();
  g0 = ld128(rs, WS_ACC + goff, 0, AUX_SC1); g1 = ld128(rs, WS_ACC + goff + 128, 0, AUX_SC1);
  if (__builtin_expect(any(stale(g0, tag) | stale(g1, tag)), 0)) {
#pragma nounroll
    for (unsigned spins = 0;;) {
      RELOAD();
      const bool s0 = !MASKED || stale(g0, tag) != 0u, s1 = !MASKED || stale(g1, tag) != 0u;
      const u32x4 n0 = ld128(rs, s0 ? WS_ACC + goff : WS_END, 0, AUX_SC1), n1 = ld128(rs, s1 ? WS_ACC + goff + 128 : WS_END, 0, AUX_SC1);
      if (s0) g0 = n0;
      if (s1) g1 = n1;
      if (!any(stale(g0, tag) | stale(g1, tag))) break;
      if (++spins > SPIN_CAP) { ok = false; break; }
    }
  }
  ST(6);
  {
    const float od = rbf(fold4(wt * asf(g0[0]), wt * asf(g0[2]), wt * asf(g1[0]), wt * asf(g1[2])));
    const float o0 = bperm(rho, od), o1 = bperm(rho + 16, od), o2 = bperm(rho + 32, od), o3 = bperm(rho + 48, od);
    float y[8];
#pragma unroll
    for (int k = 0; k < 8; ++k) y[k] = fmaf(o3, wf[3][k], fmaf(o2, wf[2][k], fmaf(o1, wf[1][k], o0 * wf[0][k])));
    const float yv = fold8(y, b3);
    redY[w * 64 + lane] = ok ? yv : __builtin_nanf("");
  }
  wg_barrier();
  if (w >= 2) { ST(7); STDUMP(); return; }
  {
    const float* p = redY + w * 256 + 8 * kap + rho;
    const float t = (p[0] + p[64]) + (p[128] + p[192]);
    __builtin_amdgcn_raw_buffer_store_b64(u32x2{asu(t), tag}, rs, WS_Y + me + w * 512 + lane * 8, 0, AUX_SC1);
  }
  ST(7);
  if (w == 1) { STDUMP(); return; }
  float gate = 0.f;
#pragma unroll
  for (int k = 0; k < 4; ++k) { gate += rbf(lo(gx[k]) * lo(gg[k])); gate += rbf(hi(gx[k]) * hi(gg[k])); }
  gate = rbf(sumhi(sum8(gate)));
  gate = rbf(1.f / (1.f + __expf(-gate)));
  if (lane >= 32) gate = rbf(1.f - gate);
  asm volatile("" : "+v"(gate));
  SB();
  const int y_off = WS_Y + ((lane & 31) * 8 + s) * 1024 + (lane >> 5) * 512 + h * 16;
  g0 = ld128(rs, y_off, 0, AUX_SC1);
#pragma nounroll
  for (unsigned spins = 0; any(stale(g0, tag));) {
    RELOAD();
    const bool s0 = !MASKED || stale(g0, tag) != 0u;
    const u32x4 n0 = ld128(rs, s0 ? y_off : WS_END, 0, AUX_SC1);
    if (s0) g0 = n0;
    if (++spins > SPIN_CAP) { ok = false; break; }
  }
  ST(8);
  {
    float v0 = sum8(asf(g0[0])), v1 = sum8(asf(g0[2]));
    v0 += dpp<ROR8>(v0); v1 += dpp<ROR8>(v1);
    float t = rbf(tr16(v0, v1));
    t = rbf(gate * t);
    t = tr32(t, t);
    auto r2 = __builtin_amdgcn_permlane16_swap(asu(t), asu(t), false, false);
    const unsigned o = ok ? pkbf(asf(r2[0]), asf(r2[1])) : 0x7fc07fc0u;
    __builtin_amdgcn_raw_buffer_store_b32(o, mk((const void*)ka1[1], D * 2), lane == 0 ? (32 * s + h) * 4 : D * 2, 0, 0);
    if (b == 0) __builtin_amdgcn_raw_buffer_store_b32(tag, rs, lane == 0 ? WS_EPOCH : WS_END, 0, AUX_SC1);
  }
  ST(9);
  STDUMP();
  asm volatile(".Lcode_to:");
}

extern "C" void kernel_launch(void* const* d_in, const int* in_sizes, int n_in, void* d_out, int out_size, void* d_ws, size_t ws_size,
                              hipStream_t stream) {
  (void)hipFuncSetAttribute((const void*)fused_decode, hipFuncAttributeMaxDynamicSharedMemorySize, LDS_BYTES);
  fused_decode<<<256, 256, LDS_BYTES, stream>>>(d_in[0], d_in[1], d_in[2], d_in[3], d_in[4], d_in[6], d_in[7], d_in[8], d_in[9], d_out, d_ws);
}
```

```cpp
#include <hip/hip_runtime.h>
#include <hip/hip_bf16.h>
#include <cstdint>
#include <utility>

constexpr int TAU = 3686;
constexpr int TOFF = 60;
constexpr int LEAD = 16;
constexpr int LEND = 10;
constexpr int CATCH = 4;
constexpr int JE = 4, JT = 12;
constexpr int J0 = 16;
constexpr int JU = 22;
constexpr int J1 = 32;
constexpr int JC = 38;
constexpr int J2 = 44;
constexpr int JL = 48, JG = 62, JGA = 83;
constexpr int JWARM = 108;
constexpr int RING = J2 + 1;
constexpr int OSHIFT = 10, GOV_THR = 40, GOV_FREE = 1500, GOV_MAX = 140;
constexpr float GOV_GAIN = 0.025f;

template <int A, int... I, class Fn> __device__ __forceinline__ void static_for_impl(std::integer_sequence<int, I...>, Fn&& f) { (f(std::integral_constant<int, A + I>{}), ...); }
template <int A, int B, class Fn> __device__ __forceinline__ void static_for(Fn&& f) { if constexpr (B > A) static_for_impl<A>(std::make_integer_sequence<int, B - A>{}, f); }
constexpr int cmin(int a, int b) { return a < b ? a : b; }
constexpr int cmax(int a, int b) { return a > b ? a : b; }
constexpr int cons(int j) { return j < J2 ? 0 : cmin(CATCH * (j - J2 + 1), cmax(cmax(j + 1 - LEAD, 128 - LEND - 2 * (127 - j)), 0)); }
constexpr int first_slot_with(int n) { int j = 0; while (cons(j) < n) ++j; return j; }
constexpr int slot_ticks(int j) { return ((j + (j >= JU ? 3 : 0)) * TAU) >> 8; }

typedef unsigned u32x4 __attribute__((ext_vector_type(4)));
typedef unsigned u32x2 __attribute__((ext_vector_type(2)));
typedef unsigned long long u64x8 __attribute__((ext_vector_type(8)));
typedef unsigned long long u64x2 __attribute__((ext_vector_type(2)));
typedef float f32x2 __attribute__((ext_vector_type(2)));
typedef __bf16 bf16x2 __attribute__((ext_vector_type(2)));
typedef __amdgpu_buffer_rsrc_t rsrc_t;

constexpr int D = 512, H = 32, SK = 8192, DH = 128, F = 1376;
constexpr int AUX_NT = 2, AUX_SC1 = 16, RSRC_W3 = 0x00020000;
constexpr int WS_EPOCH = 0;
constexpr int WS_T0 = 1024;
constexpr int WS_LATE = 2048;
constexpr int WS_ML = 4096;
constexpr int WS_Q = 65536;
constexpr int WS_GU = WS_Q + 256 * 1024;
constexpr int WS_ACC = WS_GU + 256 * 1024;
constexpr int WS_Y = WS_ACC + 256 * 1024;
constexpr int WS_END = WS_Y + 256 * 1024;
constexpr unsigned SPIN_CAP = 1u << 18;
constexpr int LDS_BYTES = 96 * 1024;

__device__ __forceinline__ float lo(unsigned u) { return __builtin_bit_cast(float, u << 16); }
__device__ __forceinline__ float hi(unsigned u) { return __builtin_bit_cast(float, u & 0xffff0000u); }
__device__ __forceinline__ float asf(unsigned u) { return __builtin_bit_cast(float, u); }
__device__ __forceinline__ unsigned asu(float f) { return __builtin_bit_cast(unsigned, f); }
__device__ __forceinline__ unsigned pkbf(float a, float b) {
  bf16x2 v = {(__bf16)a, (__bf16)b};
  return __builtin_bit_cast(unsigned, v);
}
__device__ __forceinline__ float rbf(float f) { return lo(pkbf(f, f)); }
__device__ __forceinline__ float expd(float a, float b) { return __builtin_amdgcn_exp2f((a - b) * 1.4426950408889634f); }
__device__ __forceinline__ float dot2(unsigned k, unsigned q, float acc) {
  return __builtin_amdgcn_fdot2_f32_bf16(__builtin_bit_cast(bf16x2, k), __builtin_bit_cast(bf16x2, q), acc, false);
}
template <int CTRL> __device__ __forceinline__ int dppi(int x) { return __builtin_amdgcn_mov_dpp(x, CTRL, 0xf, 0xf, true); }
template <int CTRL> __device__ __forceinline__ float dpp(float x) { return __builtin_bit_cast(float, dppi<CTRL>(__builtin_bit_cast(int, x))); }
constexpr int XOR1 = 0xB1, XOR2 = 0x4E, XOR7 = 0x141, ROR8 = 0x128, ROR4 = 0x124, ROR12 = 0x12C, BCAST = 0x150;
__device__ __forceinline__ float sum8(float x) { x += dpp<XOR1>(x); x += dpp<XOR2>(x); x += dpp<XOR7>(x); return x; }
__device__ __forceinline__ float max8(float x) { x = fmaxf(x, dpp<XOR1>(x)); x = fmaxf(x, dpp<XOR2>(x)); x = fmaxf(x, dpp<XOR7>(x)); return x; }
__device__ __forceinline__ float tr32(float a, float b) {
  auto r = __builtin_amdgcn_permlane32_swap(asu(a), asu(b), false, false);
  return asf(r[0]) + asf(r[1]);
}
__device__ __forceinline__ float tr16(float a, float b) {
  auto r = __builtin_amdgcn_permlane16_swap(asu(a), asu(b), false, false);
  return asf(r[0]) + asf(r[1]);
}
__device__ __forceinline__ float maxhi(float a) {
  a = fmaxf(a, dpp<ROR8>(a));
  auto r = __builtin_amdgcn_permlane16_swap(asu(a), asu(a), false, false);
  a = fmaxf(asf(r[0]), asf(r[1]));
  auto t = __builtin_amdgcn_permlane32_swap(asu(a), asu(a), false, false);
  return fmaxf(asf(t[0]), asf(t[1]));
}
__device__ __forceinline__ float sumhi(float x) { x += dpp<ROR8>(x); x = tr16(x, x); return tr32(x, x); }
__device__ __forceinline__ float fold4(float a0, float a1, float b0, float b1) { float x = tr16(tr32(a0, b0), tr32(a1, b1)); return x + dpp<ROR8>(x); }
__device__ __forceinline__ float fold8(const float (&y)[8], bool b3) {
  const float t0 = tr32(y[0], y[4]), t1 = tr32(y[1], y[5]), t2 = tr32(y[2], y[6]), t3 = tr32(y[3], y[7]);
  const float u0 = tr16(t0, t2), u1 = tr16(t1, t3);
  return (b3 ? u1 : u0) + dpp<ROR8>(b3 ? u0 : u1);
}
__device__ __forceinline__ f32x2 fold8to2(const float (&y)[8]) {
  const float t0 = tr32(y[0], y[4]), t1 = tr32(y[1], y[5]), t2 = tr32(y[2], y[6]), t3 = tr32(y[3], y[7]);
  return f32x2{tr16(t0, t2), tr16(t1, t3)};
}
__device__ __forceinline__ float bperm(int src_lane, float x) { return asf(__builtin_amdgcn_ds_bpermute(src_lane * 4, asu(x))); }
__device__ __forceinline__ void wg_barrier() {
  __builtin_amdgcn_fence(__ATOMIC_RELEASE, "workgroup");
  __builtin_amdgcn_s_barrier();
  __builtin_amdgcn_fence(__ATOMIC_ACQUIRE, "workgroup");
}
__device__ __forceinline__ rsrc_t mk(const void* p, int bytes) { return __builtin_amdgcn_make_buffer_rsrc((void*)p, 0, bytes, RSRC_W3); }
#define ld128(r, voff, soff, aux) __builtin_amdgcn_raw_buffer_load_b128((r), (voff), (soff), (aux))
__device__ __forceinline__ unsigned stale(u32x4 g, unsigned tag) { return (g[1] ^ tag) | (g[3] ^ tag); }
__device__ __forceinline__ bool any(unsigned v) { return __builtin_amdgcn_ballot_w64(v != 0u) != 0ull; }
#define SB() __builtin_amdgcn_sched_barrier(0)
#define PIN8(a) asm volatile("" : "+v"(a[0]), "+v"(a[1]), "+v"(a[2]), "+v"(a[3]), "+v"(a[4]), "+v"(a[5]), "+v"(a[6]), "+v"(a[7]))
#define RELOAD() asm volatile("" ::: "memory")

__global__ void __launch_bounds__(256, 1)
fused_decode(const void* a_x, const void* a_k, const void* a_v, const void* a_wq, const void* a_wo, const void* a_wg,
             const void* a_wu, const void* a_wd, const void* a_gw, void* a_out, void* a_ws) {
  u64x8 ka0; u64x2 ka1; unsigned long long ka2, tentry, pc; unsigned code, ctmp;
  asm volatile(
      "s_load_dwordx16 %[a0], %[ka], 0x0\n\t"
      "s_memrealtime %[t]\n\t"
      "s_getpc_b64 %[pc]\n"
      ".Lcode_from:\n\t"
      "s_lshr_b32 %[c], %[wg], 3\n\t"
      "s_lshl_b32 %[c], %[c], 10\n\t"
      "v_lshl_add_u32 %[v], %[tid], 2, %[c]\n\t"
      "v_min_u32 %[v], .Lcode_to-.Lcode_from-4, %[v]\n\t"
      "global_load_dword %[v], %[v], %[pc]\n\t"
      "s_load_dwordx4 %[a1], %[ka], 0x40\n\t"
      "s_load_dwordx2 %[a2], %[ka], 0x50"
      : [a0] "=&s"(ka0), [a1] "=&s"(ka1), [a2] "=&s"(ka2), [t] "=&s"(tentry), [pc] "=&s"(pc), [c] "=&s"(ctmp), [v] "=&v"(code)
      : [ka] "s"(__builtin_amdgcn_kernarg_segment_ptr()), [wg] "s"(blockIdx.x), [tid] "v"(threadIdx.x)
      : "scc", "memory");
  extern __shared__ __attribute__((aligned(16))) float lds[];
  float* const redQ = lds;
  float* const redG = lds + 512;
  float* const redA = lds + 1024;
  unsigned short* const qs = (unsigned short*)(lds + 1536);
  float* const sml = lds + 1600;
  float* const redY = lds + 1664;

  const int b = blockIdx.x, h = (b & 7) * 4 + (b >> 6), s = (b >> 3) & 7;
  const int me = (h * 8 + s) * 1024;
  const int tid = threadIdx.x, lane = tid & 63, w = __builtin_amdgcn_readfirstlane(tid >> 6);
  const int r = lane >> 4, c = lane & 15, rho = lane >> 3, kap = lane & 7;
  const bool b0 = lane & 1, b1 = lane & 2, b2 = lane & 4, b3 = lane & 8;
  const int row0 = 64 * s + 16 * w;
  int wq_v = r * 4 * 8192 + c * 16, wq_s = row0 * 8192 + h * 256;
  asm volatile("s_waitcnt lgkmcnt(0)" : "+s"(ka0), "+s"(ka1), "+s"(ka2), "+s"(tentry), "+v"(wq_v), "+s"(wq_s) :: "memory");
  unsigned char* const ws = (unsigned char*)ka2;
  const size_t kvb = (size_t)(h * SK + s * 1024) * 256;
  const rsrc_t rs = mk(ws, WS_END), rx = mk((const void*)ka0[0], D * 2);
  const rsrc_t rk = mk((const char*)ka0[1] + kvb, 1024 * 256), rv = mk((const char*)ka0[2] + kvb, 1024 * 256);
  unsigned tstart = (unsigned)tentry + TOFF;

  u32x4 kv[RING];
  unsigned stretch = 0, tag = 0;
  u32x2 lateg;
  auto issue = [&](auto J) {
    constexpr int j = J;
    if constexpr (j % 2 == 0) {
      if constexpr (j > JGA) tstart += stretch;
      int late;
      do { late = (int)((unsigned)__builtin_amdgcn_s_memrealtime() - (tstart + slot_ticks(j))); } while (late < 0);
      if constexpr (j == JL) {
        if (late > GOV_THR && w == 0) __builtin_amdgcn_raw_buffer_store_b64(u32x2{(unsigned)late, tag}, rs, lane == 0 ? WS_LATE + b * 8 : WS_END, 0, AUX_SC1);
      }
    }
    if constexpr (j == JG) lateg = __builtin_amdgcn_raw_buffer_load_b64(rs, WS_LATE + tid * 8, 0, AUX_SC1);
    if constexpr (j < 64) kv[j % RING] = ld128(rk, tid * 16, j * 4096, AUX_NT); else kv[j % RING] = ld128(rv, tid * 16, (j - 64) * 4096, AUX_NT);
    SB();
  };
  const unsigned epoch = __builtin_amdgcn_raw_buffer_load_b32(rs, WS_EPOCH, 0, AUX_SC1);
  u32x4 wqv[4];
  {
    const rsrc_t rq = mk((const void*)ka0[3], D * H * DH * 2);
#pragma unroll
    for (int j = 0; j < 4; ++j) wqv[j] = ld128(rq, wq_v, wq_s + j * 8192, AUX_NT);
  }
  const u32x2 xq = __builtin_amdgcn_raw_buffer_load_b64(rx, 8 * r, row0 * 2, 0);
  SB();
  static_for<0, JE>(issue);
  tag = epoch * 0x9E3779B1u + 0x7F4A7C15u;
  __builtin_amdgcn_raw_buffer_store_b128(u32x4{(unsigned)tentry, tag, (unsigned)tentry, tag}, rs, (b < 8 && tid < 8) ? WS_T0 + b * 128 + lane * 16 : WS_END, 0, AUX_SC1);
  SB();
  static_for<JE, JT>(issue);
  const u32x2 t0g = __builtin_amdgcn_raw_buffer_load_b64(rs, WS_T0 + kap * 128, 0, AUX_SC1);
  SB();
  static_for<JT, J0>(issue);
  {
    float qa[8];
#pragma unroll
    for (int j = 0; j < 4; ++j) {
      const float xv = (j & 1) ? hi(xq[j >> 1]) : lo(xq[j >> 1]);
#pragma unroll
      for (int k = 0; k < 4; ++k) {
        qa[2 * k] = j ? fmaf(xv, lo(wqv[j][k]), qa[2 * k]) : xv * lo(wqv[j][k]);
        qa[2 * k + 1] = j ? fmaf(xv, hi(wqv[j][k]), qa[2 * k + 1]) : xv * hi(wqv[j][k]);
      }
    }
    *(f32x2*)(redQ + w * 128 + 2 * lane) = fold8to2(qa);
  }
  SB();
  static_for<J0, J0 + 2>(issue);
  const int pub_lds = r * 128 + 2 * (16 * (c & 3) + 4 * w + (c >> 2)), pub_off = lane < 16 ? me + (16 * w + c) * 16 : WS_END;
  auto publish_pairs = [&](const float* red, int base, bool on) {
    const f32x2 a = *(const f32x2*)(red + pub_lds);
    float t = tr32(a[0], a[1]);
    t = tr16(t, t);
    auto r2 = __builtin_amdgcn_permlane32_swap(asu(t), asu(t), false, false);
    __builtin_amdgcn_raw_buffer_store_b128(u32x4{r2[0], tag, r2[1], tag}, rs, on ? pub_off : WS_END, base, AUX_SC1);
    SB();
  };
  wg_barrier();
  publish_pairs(redQ, WS_Q, true);
  static_for<J0 + 2, JT + 10>(issue);
  {
    const bool have = t0g[1] == tag;
    const float dsum = sum8(have ? (float)(int)(t0g[0] - (unsigned)tentry) : 0.f), cnt = sum8(have ? 1.f : 0.f);
    const float shift = fminf(fmaxf(dsum / fmaxf(cnt, 1.f) - (cnt > 0.f ? (float)OSHIFT : 0.f), -300.f), 100.f);
    tstart += __builtin_amdgcn_readfirstlane((int)shift);
    SB();
  }
  static_for<JT + 10, JU>(issue);
  u32x4 gv[2], uv[2];
  {
    const rsrc_t rg = mk((const void*)ka0[5], D * F * 2), ru = mk((const void*)ka0[6], D * F * 2);
    const int chunk = 6 * h + kap;
    const int go = (kap < 6 && chunk < F / 8) ? rho * (F * 2) + chunk * 16 : D * F * 2;
#pragma unroll
    for (int t = 0; t < 2; ++t) { gv[t] = ld128(rg, go, (row0 + 8 * t) * (F * 2), AUX_NT); uv[t] = ld128(ru, go, (row0 + 8 * t) * (F * 2), AUX_NT); }
  }
  const unsigned short xg0 = __builtin_amdgcn_raw_buffer_load_b16(rx, 2 * rho, row0 * 2, 0);
  const unsigned short xg1 = __builtin_amdgcn_raw_buffer_load_b16(rx, 2 * rho + 16, row0 * 2, 0);
  SB();
  static_for<JU, J1>(issue);
  const int goff = (h * 8 + rho) * 1024 + w * 256 + kap * 16;
  u32x4 g0 = ld128(rs, WS_Q + goff, 0, AUX_SC1), g1 = ld128(rs, WS_Q + goff + 128, 0, AUX_SC1);
  SB();
  static_for<J1, JC>(issue);
  {
    float p[16];
    const float x0 = lo(xg0), x1 = lo(xg1);
#pragma unroll
    for (int k = 0; k < 4; ++k) {
      p[2 * k] = fmaf(x1, lo(gv[1][k]), x0 * lo(gv[0][k])); p[2 * k + 1] = fmaf(x1, hi(gv[1][k]), x0 * hi(gv[0][k]));
      p[8 + 2 * k] = fmaf(x1, lo(uv[1][k]), x0 * lo(uv[0][k])); p[9 + 2 * k] = fmaf(x1, hi(uv[1][k]), x0 * hi(uv[0][k]));
    }
#pragma unroll
    for (int k = 0; k < 8; ++k) p[k] = tr32(p[k], p[k + 8]);
#pragma unroll
    for (int k = 0; k < 4; ++k) p[k] = tr16(p[k], p[k + 4]);
#pragma unroll
    for (int k = 0; k < 2; ++k) p[k] = (b3 ? p[k + 2] : p[k]) + dpp<ROR8>(b3 ? p[k] : p[k + 2]);
    const int slot = 8 * kap + rho;
    *(f32x2*)(redG + w * 128 + 2 * (16 * (slot & 3) + (slot >> 2))) = f32x2{p[0], p[1]};
    SB();
  }
  static_for<JC, J2>(issue);
  bool ok = true;
  asm volatile("" :: "v"(code));
  if (__builtin_expect(any(stale(g0, tag) | stale(g1, tag)), 0)) {
#pragma nounroll
    for (unsigned spins = 0;;) {
      RELOAD();
      g0 = ld128(rs, WS_Q + goff, 0, AUX_SC1); g1 = ld128(rs, WS_Q + goff + 128, 0, AUX_SC1);
      if (!any(stale(g0, tag) | stale(g1, tag))) break;
      if (++spins > SPIN_CAP) { ok = false; break; }
    }
  }
  {
    const float qd = fold4(asf(g0[0]), asf(g0[2]), asf(g1[0]), asf(g1[2]));
    qs[32 * w + (lane >> 5) * 16 + 2 * kap + (r & 1)] = (unsigned short)pkbf(qd, qd);
  }
  wg_barrier();
  const u32x4 qv = *(const u32x4*)(qs + 8 * c);
  publish_pairs(redG, WS_GU, w < 3);

  float sc[4], f1, f2, f4, f8;
  auto eat_k = [&](auto I) {
    constexpr int i = I;
    const u32x4 k = kv[i % RING];
    float d = dot2(k[0], qv[0], 0.f); d = dot2(k[1], qv[1], d); d = dot2(k[2], qv[2], d); d = dot2(k[3], qv[3], d);
    if constexpr (!(i & 1)) f1 = d;
    else {
      d = (b0 ? d : f1) + dpp<XOR1>(b0 ? f1 : d);
      if constexpr (!(i & 2)) f2 = d;
      else {
        d = (b1 ? d : f2) + dpp<XOR2>(b1 ? f2 : d);
        if constexpr (!(i & 4)) f4 = d;
        else {
          const float t1 = f4 + dpp<ROR12>(f4), t2 = d + dpp<ROR4>(d);
          d = b2 ? t2 : t1;
          if constexpr (!(i & 8)) f8 = d;
          else { d = (b3 ? d : f8) + dpp<ROR8>(b3 ? f8 : d); sc[i >> 4] = rbf(rbf(d) * 0.08838834764831845f); }
        }
      }
    }
    asm volatile("" : "+v"(d));
    SB();
  };
  constexpr int JK = first_slot_with(64);
  static_assert(JE < JT && JT <= J0 && J0 + 2 <= JT + 10 && JT + 10 <= JU && JU <= J1 && J1 <= JC && JC <= J2 && cons(JK) == 64 && LEAD < RING && LEND >= 6 && JK + 3 < JWARM && JWARM + LEAD < 128, "schedule");
  static_for<J2, JK + 1>([&](auto J) { constexpr int j = J; issue(J); static_for<cons(j - 1), cons(j)>(eat_k); });
  const float m = fmaxf(maxhi(max8(fmaxf(fmaxf(sc[0], sc[1]), fmaxf(sc[2], sc[3])))), -3.0e38f);
#pragma unroll
  for (int i = 0; i < 4; ++i) sc[i] = expd(sc[i], m);
  const float l = sumhi(sum8((sc[0] + sc[1]) + (sc[2] + sc[3])));
  const float lsum = sumhi(sum8(lateg[1] == tag ? (float)(int)lateg[0] : 0.f));
  if (lane == 0) { *(f32x2*)(sml + 2 * w) = f32x2{m, l}; sml[8 + w] = lsum; }

  float acc[8] = {0.f, 0.f, 0.f, 0.f, 0.f, 0.f, 0.f, 0.f};
  auto eat_v = [&](auto I) {
    constexpr int i = I - 64;
    const u32x4 v = kv[(64 + i) % RING];
    const float p = dpp<BCAST + (i & 15)>(sc[i >> 4]);
#pragma unroll
    for (int k = 0; k < 4; ++k) { acc[2 * k] = fmaf(p, lo(v[k]), acc[2 * k]); acc[2 * k + 1] = fmaf(p, hi(v[k]), acc[2 * k + 1]); }
    PIN8(acc);
    SB();
  };
  u32x4 wt8[6], warm;
  const rsrc_t ro = mk((const void*)ka0[4], H * DH * D * 2), rd = mk((const void*)ka0[7], F * D * 2);
  const int dro = (2 * (rho & 3) + (rho >> 2)) * 1024 + kap * 16 + s * 128;
  auto tile = [&](auto T) {
    constexpr int t = T;
    if constexpr (t < 4) wt8[t] = ld128(ro, rho * 2048 + kap * 16, (h * 128 + 32 * w + 16 * (t >> 1) + (t & 1)) * 1024 + s * 128, AUX_NT);
    else if constexpr (t == 4) wt8[4] = ld128(rd, dro + (6 * h + w) * 8192, 0, AUX_NT);
    else wt8[5] = ld128(rd, w < 2 ? dro + (6 * h + w + 4) * 8192 : F * D * 2, 0, AUX_NT);
    SB();
  };
  auto step_v = [&](auto J) {
    constexpr int j = J;
    issue(J);
    if constexpr (j == JWARM) {
      warm = ld128(rs, r == 0 ? WS_ACC + me + w * 256 + c * 16 : r == 1 ? WS_Y + me + w * 256 + c * 16 : WS_END, 0, AUX_SC1);
      SB();
    }
    static_for<cons(j - 1), cons(j)>(eat_v);
  };
  static_for<JK + 1, JK + 3>(step_v);
  wg_barrier();
  float fw;
  {
    const f32x2 ml = *(const f32x2*)(sml + 2 * (lane & 3));
    float M = ml[0];
    M = fmaxf(M, dpp<XOR1>(M)); M = fmaxf(M, dpp<XOR2>(M));
    fw = expd(m, M);
    float L = ml[1] * expd(ml[0], M);
    L += dpp<XOR1>(L); L += dpp<XOR2>(L);
    __builtin_amdgcn_raw_buffer_store_b128(u32x4{asu(M), tag, asu(L), tag}, rs, tid < 8 ? WS_ML + (h * 8 + s) * 128 + tid * 16 : WS_END, 0, AUX_SC1);
    float lm = sml[8 + (lane & 3)];
    lm += dpp<XOR1>(lm); lm += dpp<XOR2>(lm);
    stretch = __builtin_amdgcn_readfirstlane((int)(fminf(fmaxf(lm - (float)GOV_FREE, 0.f) * GOV_GAIN, (float)GOV_MAX) * (2.f / (127 - JGA)) + 0.5f));
    asm volatile("" : "+v"(fw));
    SB();
  }
  static_for<JK + 3, 128>(step_v);
  asm volatile("" :: "v"(warm));
  const int gu_off = WS_GU + (h * 8 + rho) * 1024 + w * 128 + kap * 16;
  const int gu_off1 = w < 2 ? gu_off + 512 : WS_END;
  const unsigned tag1 = w < 2 ? tag : 0u;
  const int ml_off = WS_ML + (h * 8 + rho) * 128 + kap * 16;
  tile(std::integral_constant<int, 5>{}); tile(std::integral_constant<int, 4>{});
  const u32x4 gx = ld128(rx, w == 0 ? lane * 16 : D * 2, 0, 0), gg = ld128(mk((const void*)ka1[0], D * 2), w == 0 ? lane * 16 : D * 2, 0, 0);
  u32x4 gu0 = ld128(rs, gu_off, 0, AUX_SC1), gu1 = ld128(rs, gu_off1, 0, AUX_SC1), mlg = ld128(rs, ml_off, 0, AUX_SC1);
  SB();
  static_for<0, 4>([&](auto T) { constexpr int t = T; tile(T); eat_v(std::integral_constant<int, cons(127) + t>{}); });
  static_for<cons(127) + 4, 128>(eat_v);
  *(f32x2*)(redA + w * 128 + 2 * lane) = fold8to2(acc) * fw;
  wg_barrier();
  publish_pairs(redA, WS_ACC, true);
  if (__builtin_expect(any(stale(gu0, tag) | stale(gu1, tag1) | stale(mlg, tag)), 0)) {
#pragma nounroll
    for (unsigned spins = 0;;) {
      RELOAD();
      gu0 = ld128(rs, gu_off, 0, AUX_SC1); gu1 = ld128(rs, gu_off1, 0, AUX_SC1); mlg = ld128(rs, ml_off, 0, AUX_SC1);
      if (!any(stale(gu0, tag) | stale(gu1, tag1) | stale(mlg, tag))) break;
      if (++spins > SPIN_CAP) { ok = false; break; }
    }
  }
  float wt;
  {
    const float ms = asf(mlg[0]);
    wt = expd(ms, maxhi(ms));
    wt *= __builtin_amdgcn_rcpf(sumhi(wt * asf(mlg[2])));
    const float gu = rbf(fold4(asf(gu0[0]), asf(gu0[2]), asf(gu1[0]), asf(gu1[2])));
    const int src = (rho & 3) + 16 * (rho >> 2);
    const float h0 = rbf(bperm(src, gu) * bperm(src + 4, gu)), h1 = rbf(bperm(src + 32, gu) * bperm(src + 36, gu));
    float y[8];
#pragma unroll
    for (int k = 0; k < 4; ++k) { y[2 * k] = fmaf(h1, lo(wt8[5][k]), h0 * lo(wt8[4][k])); y[2 * k + 1] = fmaf(h1, hi(wt8[5][k]), h0 * hi(wt8[4][k])); }
    redY[256 + w * 64 + lane] = fold8(y, b3);
  }
  float wf[4][8];
#pragma unroll
  for (int t = 0; t < 4; ++t) {
#pragma unroll
    for (int k = 0; k < 4; ++k) { wf[t][2 * k] = lo(wt8[t][k]); wf[t][2 * k + 1] = hi(wt8[t][k]); }
    PIN8(wf[t]);
  }
  asm volatile("" : "+v"(wt));
  SB();
  g0 = ld128(rs, WS_ACC + goff, 0, AUX_SC1); g1 = ld128(rs, WS_ACC + goff + 128, 0, AUX_SC1);
  if (__builtin_expect(any(stale(g0, tag) | stale(g1, tag)), 0)) {
#pragma nounroll
    for (unsigned spins = 0;;) {
      RELOAD();
      const bool s0 = stale(g0, tag) != 0u, s1 = stale(g1, tag) != 0u;
      const u32x4 n0 = ld128(rs, s0 ? WS_ACC + goff : WS_END, 0, AUX_SC1), n1 = ld128(rs, s1 ? WS_ACC + goff + 128 : WS_END, 0, AUX_SC1);
      if (s0) g0 = n0;
      if (s1) g1 = n1;
      if (!any(stale(g0, tag) | stale(g1, tag))) break;
      if (++spins > SPIN_CAP) { ok = false; break; }
    }
  }
  {
    const float od = rbf(fold4(wt * asf(g0[0]), wt * asf(g0[2]), wt * asf(g1[0]), wt * asf(g1[2])));
    const float o0 = bperm(rho, od), o1 = bperm(rho + 16, od), o2 = bperm(rho + 32, od), o3 = bperm(rho + 48, od);
    float y[8];
#pragma unroll
    for (int k = 0; k < 8; ++k) y[k] = fmaf(o3, wf[3][k], fmaf(o2, wf[2][k], fmaf(o1, wf[1][k], o0 * wf[0][k])));
    const float yv = fold8(y, b3);
    redY[w * 64 + lane] = ok ? yv : __builtin_nanf("");
  }
  wg_barrier();
  if (w >= 2) return;
  {
    const float* p = redY + w * 256 + 8 * kap + rho;
    const float t = (p[0] + p[64]) + (p[128] + p[192]);
    __builtin_amdgcn_raw_buffer_store_b64(u32x2{asu(t), tag}, rs, WS_Y + me + w * 512 + lane * 8, 0, AUX_SC1);
  }
  if (w == 1) return;
  float gate = 0.f;
#pragma unroll
  for (int k = 0; k < 4; ++k) { gate += rbf(lo(gx[k]) * lo(gg[k])); gate += rbf(hi(gx[k]) * hi(gg[k])); }
  gate = rbf(sumhi(sum8(gate)));
  gate = rbf(1.f / (1.f + __expf(-gate)));
  if (lane >= 32) gate = rbf(1.f - gate);
  asm volatile("" : "+v"(gate));
  SB();
  const int y_off = WS_Y + ((lane & 31) * 8 + s) * 1024 + (lane >> 5) * 512 + h * 16;
  g0 = ld128(rs, y_off, 0, AUX_SC1);
#pragma nounroll
  for (unsigned spins = 0; any(stale(g0, tag));) {
    RELOAD();
    const bool s0 = stale(g0, tag) != 0u;
    const u32x4 n0 = ld128(rs, s0 ? y_off : WS_END, 0, AUX_SC1);
    if (s0) g0 = n0;
    if (++spins > SPIN_CAP) { ok = false; break; }
  }
  {
    float v0 = sum8(asf(g0[0])), v1 = sum8(asf(g0[2]));
    v0 += dpp<ROR8>(v0); v1 += dpp<ROR8>(v1);
    float t = rbf(tr16(v0, v1));
    t = rbf(gate * t);
    t = tr32(t, t);
    auto r2 = __builtin_amdgcn_permlane16_swap(asu(t), asu(t), false, false);
    const unsigned o = ok ? pkbf(asf(r2[0]), asf(r2[1])) : 0x7fc07fc0u;
    __builtin_amdgcn_raw_buffer_store_b32(o, mk((const void*)ka1[1], D * 2), lane == 0 ? (32 * s + h) * 4 : D * 2, 0, 0);
    if (b == 0) __builtin_amdgcn_raw_buffer_store_b32(tag, rs, lane == 0 ? WS_EPOCH : WS_END, 0, AUX_SC1);
  }
  asm volatile(".Lcode_to:");
}

extern "C" void kernel_launch(void* const* d_in, const int* in_sizes, int n_in, void* d_out, int out_size, void* d_ws, size_t ws_size,
                              hipStream_t stream) {
  (void)hipFuncSetAttribute((const void*)fused_decode, hipFuncAttributeMaxDynamicSharedMemorySize, LDS_BYTES);
  fused_decode<<<256, 256, LDS_BYTES, stream>>>(d_in[0], d_in[1], d_in[2], d_in[3], d_in[4], d_in[6], d_in[7], d_in[8], d_in[9], d_out, d_ws);
}
```

```cpp
#define DIAG 1
#include <hip/hip_runtime.h>
#include <hip/hip_bf16.h>
#include <cstdint>
#include <utility>

#define STAMP 0
#define TAU 3686
#define TOFF 60
#define JE 2
#define OSHIFT 10
#define HINT_AUX 16
constexpr int LEAD = 16;
constexpr int LEND = 10;
constexpr int CATCH = 4;
constexpr int J0 = 16;
constexpr int JU = 22;
constexpr int J1 = 32;
constexpr int JC = 38;
constexpr int J2 = 44;
constexpr int JL = 48, JG = 62, JGA = 83;
constexpr int JWARM = 108;
constexpr int RING = J2 + 1;
constexpr int GOV_THR = 40, GOV_FREE = 1500, GOV_MAX = 140;
constexpr float GOV_GAIN = 0.025f;

template <int A, int... I, class Fn> __device__ __forceinline__ void static_for_impl(std::integer_sequence<int, I...>, Fn&& f) { (f(std::integral_constant<int, A + I>{}), ...); }
template <int A, int B, class Fn> __device__ __forceinline__ void static_for(Fn&& f) { if constexpr (B > A) static_for_impl<A>(std::make_integer_sequence<int, B - A>{}, f); }
constexpr int cmin(int a, int b) { return a < b ? a : b; }
constexpr int cmax(int a, int b) { return a > b ? a : b; }
constexpr int cons(int j) { return j < J2 ? 0 : cmin(CATCH * (j - J2 + 1), cmax(cmax(j + 1 - LEAD, 128 - LEND - 2 * (127 - j)), 0)); }
constexpr int first_slot_with(int n) { int j = 0; while (cons(j) < n) ++j; return j; }
constexpr int slot_ticks(int j) { return ((j + (j >= JU ? 3 : 0)) * TAU) >> 8; }

typedef unsigned u32x4 __attribute__((ext_vector_type(4)));
typedef unsigned u32x2 __attribute__((ext_vector_type(2)));
typedef unsigned long long u64x8 __attribute__((ext_vector_type(8)));
typedef unsigned long long u64x2 __attribute__((ext_vector_type(2)));
typedef float f32x2 __attribute__((ext_vector_type(2)));
typedef __bf16 bf16x2 __attribute__((ext_vector_type(2)));
typedef __amdgpu_buffer_rsrc_t rsrc_t;
#define GAS __attribute__((address_space(1)))

constexpr int D = 512, H = 32, SK = 8192, DH = 128, F = 1376;
constexpr int AUX_NT = 2, AUX_SC1 = 16, RSRC_W3 = 0x00020000;
constexpr int WS_EPOCH = 0;
constexpr int WS_T0 = 1024;
constexpr int WS_LATE = 2048;
constexpr int WS_ML = 4096;
constexpr int WS_Q = 65536;
constexpr int WS_GU = WS_Q + 256 * 1024;
constexpr int WS_ACC = WS_GU + 256 * 1024;
constexpr int WS_Y = WS_ACC + 256 * 1024;
constexpr int WS_END = WS_Y + 256 * 1024;
constexpr int WS_STAMP = 4 << 20;
constexpr unsigned SPIN_CAP = 1u << 18;
constexpr int LDS_BYTES = 96 * 1024;

__device__ __forceinline__ float lo(unsigned u) { return __builtin_bit_cast(float, u << 16); }
__device__ __forceinline__ float hi(unsigned u) { return __builtin_bit_cast(float, u & 0xffff0000u); }
__device__ __forceinline__ float asf(unsigned u) { return __builtin_bit_cast(float, u); }
__device__ __forceinline__ unsigned asu(float f) { return __builtin_bit_cast(unsigned, f); }
__device__ __forceinline__ unsigned pkbf(float a, float b) {
  bf16x2 v = {(__bf16)a, (__bf16)b};
  return __builtin_bit_cast(unsigned, v);
}
__device__ __forceinline__ float rbf(float f) { return lo(pkbf(f, f)); }
__device__ __forceinline__ float expd(float a, float b) { return __builtin_amdgcn_exp2f((a - b) * 1.4426950408889634f); }
__device__ __forceinline__ float dot2(unsigned k, unsigned q, float acc) {
  return __builtin_amdgcn_fdot2_f32_bf16(__builtin_bit_cast(bf16x2, k), __builtin_bit_cast(bf16x2, q), acc, false);
}
template <int CTRL> __device__ __forceinline__ int dppi(int x) { return __builtin_amdgcn_mov_dpp(x, CTRL, 0xf, 0xf, true); }
template <int CTRL> __device__ __forceinline__ float dpp(float x) { return __builtin_bit_cast(float, dppi<CTRL>(__builtin_bit_cast(int, x))); }
constexpr int XOR1 = 0xB1, XOR2 = 0x4E, XOR7 = 0x141, ROR8 = 0x128, ROR4 = 0x124, ROR12 = 0x12C, BCAST = 0x150;
__device__ __forceinline__ float sum8(float x) { x += dpp<XOR1>(x); x += dpp<XOR2>(x); x += dpp<XOR7>(x); return x; }
__device__ __forceinline__ float max8(float x) { x = fmaxf(x, dpp<XOR1>(x)); x = fmaxf(x, dpp<XOR2>(x)); x = fmaxf(x, dpp<XOR7>(x)); return x; }
__device__ __forceinline__ float tr32(float a, float b) {
  auto r = __builtin_amdgcn_permlane32_swap(asu(a), asu(b), false, false);
  return asf(r[0]) + asf(r[1]);
}
__device__ __forceinline__ float tr16(float a, float b) {
  auto r = __builtin_amdgcn_permlane16_swap(asu(a), asu(b), false, false);
  return asf(r[0]) + asf(r[1]);
}
__device__ __forceinline__ float maxhi(float a) {
  a = fmaxf(a, dpp<ROR8>(a));
  auto r = __builtin_amdgcn_permlane16_swap(asu(a), asu(a), false, false);
  a = fmaxf(asf(r[0]), asf(r[1]));
  auto t = __builtin_amdgcn_permlane32_swap(asu(a), asu(a), false, false);
  return fmaxf(asf(t[0]), asf(t[1]));
}
__device__ __forceinline__ float sumhi(float x) { x += dpp<ROR8>(x); x = tr16(x, x); return tr32(x, x); }
__device__ __forceinline__ float fold4(float a0, float a1, float b0, float b1) { float x = tr16(tr32(a0, b0), tr32(a1, b1)); return x + dpp<ROR8>(x); }
__device__ __forceinline__ float fold8(const float (&y)[8], bool b3) {
  const float t0 = tr32(y[0], y[4]), t1 = tr32(y[1], y[5]), t2 = tr32(y[2], y[6]), t3 = tr32(y[3], y[7]);
  const float u0 = tr16(t0, t2), u1 = tr16(t1, t3);
  return (b3 ? u1 : u0) + dpp<ROR8>(b3 ? u0 : u1);
}
__device__ __forceinline__ f32x2 fold8to2(const float (&y)[8]) {
  const float t0 = tr32(y[0], y[4]), t1 = tr32(y[1], y[5]), t2 = tr32(y[2], y[6]), t3 = tr32(y[3], y[7]);
  return f32x2{tr16(t0, t2), tr16(t1, t3)};
}
__device__ __forceinline__ float bperm(int src_lane, float x) { return asf(__builtin_amdgcn_ds_bpermute(src_lane * 4, asu(x))); }
__device__ __forceinline__ void wg_barrier() {
  __builtin_amdgcn_fence(__ATOMIC_RELEASE, "workgroup");
  __builtin_amdgcn_s_barrier();
  __builtin_amdgcn_fence(__ATOMIC_ACQUIRE, "workgroup");
}
__device__ __forceinline__ rsrc_t mk(const void* p, int bytes) { return __builtin_amdgcn_make_buffer_rsrc((void*)p, 0, bytes, RSRC_W3); }
#define ld128(r, voff, soff, aux) __builtin_amdgcn_raw_buffer_load_b128((r), (voff), (soff), (aux))
__device__ __forceinline__ unsigned stale(u32x4 g, unsigned tag) { return (g[1] ^ tag) | (g[3] ^ tag); }
__device__ __forceinline__ bool any(unsigned v) { return __builtin_amdgcn_ballot_w64(v != 0u) != 0ull; }
#define SB() __builtin_amdgcn_sched_barrier(0)
#define PIN8(a) asm volatile("" : "+v"(a[0]), "+v"(a[1]), "+v"(a[2]), "+v"(a[3]), "+v"(a[4]), "+v"(a[5]), "+v"(a[6]), "+v"(a[7]))
#define RELOAD() asm volatile("" ::: "memory")
#define ST(i) do { } while (0)
#define STDUMP() do { } while (0)
#define NSTAMP 14
#define STAMP_W0_FROM 8
#define STAMP_W0_TO 9

__global__ void __launch_bounds__(256, 1)
fused_decode(const void* a_x, const void* a_k, const void* a_v, const void* a_wq, const void* a_wo, const void* a_wg,
             const void* a_wu, const void* a_wd, const void* a_gw, void* a_out, void* a_ws) {
  u64x8 ka0; u64x2 ka1; unsigned long long ka2, tentry, pc; unsigned code, ctmp;
  asm volatile(
      "s_load_dwordx16 %[a0], %[ka], 0x0\n\t"
      "s_memrealtime %[t]\n\t"
      "s_getpc_b64 %[pc]\n"
      ".Lcode_from:\n\t"
      "s_lshr_b32 %[c], %[wg], 3\n\t"
      "s_lshl_b32 %[c], %[c], 10\n\t"
      "v_lshl_add_u32 %[v], %[tid], 2, %[c]\n\t"
      "v_min_u32 %[v], .Lcode_to-.Lcode_from-4, %[v]\n\t"
      "global_load_dword %[v], %[v], %[pc]\n\t"
      "s_load_dwordx4 %[a1], %[ka], 0x40\n\t"
      "s_load_dwordx2 %[a2], %[ka], 0x50"
      : [a0] "=&s"(ka0), [a1] "=&s"(ka1), [a2] "=&s"(ka2), [t] "=&s"(tentry), [pc] "=&s"(pc), [c] "=&s"(ctmp), [v] "=&v"(code)
      : [ka] "s"(__builtin_amdgcn_kernarg_segment_ptr()), [wg] "s"(blockIdx.x), [tid] "v"(threadIdx.x)
      : "scc", "memory");
  extern __shared__ __attribute__((aligned(16))) float lds[];
  float* const redQ = lds;
  float* const redG = lds + 512;
  float* const redA = lds + 1024;
  unsigned short* const qs = (unsigned short*)(lds + 1536);
  float* const sml = lds + 1600;
  float* const redY = lds + 1664;

  const int b = blockIdx.x, h = (b & 7) * 4 + (b >> 6), s = (b >> 3) & 7;
  const int me = (h * 8 + s) * 1024;
  const int tid = threadIdx.x, lane = tid & 63, w = __builtin_amdgcn_readfirstlane(tid >> 6);
  const int r = lane >> 4, c = lane & 15, rho = lane >> 3, kap = lane & 7;
  const bool b0 = lane & 1, b1 = lane & 2, b2 = lane & 4, b3 = lane & 8;
  const int row0 = 64 * s + 16 * w;
  int wq_v = r * 4 * 8192 + c * 16, wq_s = row0 * 8192 + h * 256;
  asm volatile("s_waitcnt lgkmcnt(0)" : "+s"(ka0), "+s"(ka1), "+s"(ka2), "+s"(tentry), "+v"(wq_v), "+s"(wq_s) :: "memory");
  unsigned char* const ws = (unsigned char*)ka2;
  const size_t kvb = (size_t)(h * SK + s * 1024) * 256;
  const rsrc_t rs = mk(ws, WS_END), rx = mk((const void*)ka0[0], D * 2);
  const rsrc_t rk = mk((const char*)ka0[1] + kvb, 1024 * 256), rv = mk((const char*)ka0[2] + kvb, 1024 * 256);
  unsigned tstart = (unsigned)tentry + TOFF;

  u32x4 kv[RING];
  unsigned stretch = 0, tag = 0;
  u32x2 lateg; int diag = 0;
  auto issue = [&](auto J) {
    constexpr int j = J;
    if constexpr (j % 2 == 0) {
      if constexpr (j > JGA) tstart += stretch;
      int late;
      do { late = (int)((unsigned)__builtin_amdgcn_s_memrealtime() - (tstart + slot_ticks(j))); } while (late < 0);
      if constexpr (j == JL) {
        if (late > GOV_THR && w == 0) __builtin_amdgcn_raw_buffer_store_b64(u32x2{(unsigned)late, tag}, rs, lane == 0 ? WS_LATE + b * 8 : WS_END, 0, AUX_SC1);
      }
    }
    if constexpr (j == JG) lateg = __builtin_amdgcn_raw_buffer_load_b64(rs, WS_LATE + tid * 8, 0, HINT_AUX);
    if constexpr (j < 64) kv[j % RING] = ld128(rk, tid * 16, j * 4096, AUX_NT); else kv[j % RING] = ld128(rv, tid * 16, (j - 64) * 4096, AUX_NT);
    if constexpr (j == 47) ST(10);
    if constexpr (j == 87) ST(11);
    if constexpr (j == 107) ST(12);
    if constexpr (j == 127) ST(13);
    SB();
  };
  const unsigned epoch = __builtin_amdgcn_raw_buffer_load_b32(rs, WS_EPOCH, 0, AUX_SC1);
  u32x4 wqv[4];
  {
    const rsrc_t rq = mk((const void*)ka0[3], D * H * DH * 2);
#pragma unroll
    for (int j = 0; j < 4; ++j) wqv[j] = ld128(rq, wq_v, wq_s + j * 8192, AUX_NT);
  }
  const u32x2 xq = __builtin_amdgcn_raw_buffer_load_b64(rx, 8 * r, row0 * 2, 0);
  SB();
  static_for<0, JE>(issue);
  tag = epoch * 0x9E3779B1u + 0x7F4A7C15u;
  __builtin_amdgcn_raw_buffer_store_b128(u32x4{(unsigned)tentry, tag, (unsigned)tentry, tag}, rs, (b < 8 && tid < 8) ? WS_T0 + b * 128 + lane * 16 : WS_END, 0, AUX_SC1);
  SB();
  static_for<JE, J0>(issue);
  {
    float qa[8];
#pragma unroll
    for (int j = 0; j < 4; ++j) {
      const float xv = (j & 1) ? hi(xq[j >> 1]) : lo(xq[j >> 1]);
#pragma unroll
      for (int k = 0; k < 4; ++k) {
        qa[2 * k] = j ? fmaf(xv, lo(wqv[j][k]), qa[2 * k]) : xv * lo(wqv[j][k]);
        qa[2 * k + 1] = j ? fmaf(xv, hi(wqv[j][k]), qa[2 * k + 1]) : xv * hi(wqv[j][k]);
      }
    }
    *(f32x2*)(redQ + w * 128 + 2 * lane) = fold8to2(qa);
  }
  SB();
  static_for<J0, J0 + 2>(issue);
  const int pub_lds = r * 128 + 2 * (16 * (c & 3) + 4 * w + (c >> 2)), pub_off = lane < 16 ? me + (16 * w + c) * 16 : WS_END;
  auto publish_pairs = [&](const float* red, int base, bool on) {
    const f32x2 a = *(const f32x2*)(red + pub_lds);
    float t = tr32(a[0], a[1]);
    t = tr16(t, t);
    auto r2 = __builtin_amdgcn_permlane32_swap(asu(t), asu(t), false, false);
    __builtin_amdgcn_raw_buffer_store_b128(u32x4{r2[0], tag, r2[1], tag}, rs, on ? pub_off : WS_END, base, AUX_SC1);
    SB();
  };
  wg_barrier();
  publish_pairs(redQ, WS_Q, true);
  static_for<J0 + 2, JU>(issue);
  u32x4 gv[2], uv[2];
  {
    const rsrc_t rg = mk((const void*)ka0[5], D * F * 2), ru = mk((const void*)ka0[6], D * F * 2);
    const int chunk = 6 * h + kap;
    const int go = (kap < 6 && chunk < F / 8) ? rho * (F * 2) + chunk * 16 : D * F * 2;
#pragma unroll
    for (int t = 0; t < 2; ++t) { gv[t] = ld128(rg, go, (row0 + 8 * t) * (F * 2), AUX_NT); uv[t] = ld128(ru, go, (row0 + 8 * t) * (F * 2), AUX_NT); }
  }
  const unsigned short xg0 = __builtin_amdgcn_raw_buffer_load_b16(rx, 2 * rho, row0 * 2, 0);
  const unsigned short xg1 = __builtin_amdgcn_raw_buffer_load_b16(rx, 2 * rho + 16, row0 * 2, 0);
  const u32x2 t0g = __builtin_amdgcn_raw_buffer_load_b64(rs, WS_T0 + kap * 128, 0, HINT_AUX);
  SB();
  static_for<JU, J1>(issue);
  {
    const bool have = t0g[1] == tag;
    { const float e = have ? (float)(int)(t0g[0] - (unsigned)tentry) : 300.f; const float mx = max8(e), mn = -max8(-e);
      diag = DIAG == 0 ? __builtin_amdgcn_readfirstlane((int)(mx - mn)) : __builtin_amdgcn_readlane((int)(e - mn), DIAG); }
    const float dsum = sum8(have ? (float)(int)(t0g[0] - (unsigned)tentry) : 0.f), cnt = sum8(have ? 1.f : 0.f);
    const float shift = fminf(fmaxf(dsum / fmaxf(cnt, 1.f) - (cnt > 0.f ? (float)OSHIFT : 0.f), -300.f), 100.f);
    tstart += __builtin_amdgcn_readfirstlane((int)shift);
    SB();
  }
  const int goff = (h * 8 + rho) * 1024 + w * 256 + kap * 16;
  u32x4 g0 = ld128(rs, WS_Q + goff, 0, AUX_SC1), g1 = ld128(rs, WS_Q + goff + 128, 0, AUX_SC1);
  SB();
  static_for<J1, JC>(issue);
  {
    float p[16];
    const float x0 = lo(xg0), x1 = lo(xg1);
#pragma unroll
    for (int k = 0; k < 4; ++k) {
      p[2 * k] = fmaf(x1, lo(gv[1][k]), x0 * lo(gv[0][k])); p[2 * k + 1] = fmaf(x1, hi(gv[1][k]), x0 * hi(gv[0][k]));
      p[8 + 2 * k] = fmaf(x1, lo(uv[1][k]), x0 * lo(uv[0][k])); p[9 + 2 * k] = fmaf(x1, hi(uv[1][k]), x0 * hi(uv[0][k]));
    }
#pragma unroll
    for (int k = 0; k < 8; ++k) p[k] = tr32(p[k], p[k + 8]);
#pragma unroll
    for (int k = 0; k < 4; ++k) p[k] = tr16(p[k], p[k + 4]);
#pragma unroll
    for (int k = 0; k < 2; ++k) p[k] = (b3 ? p[k + 2] : p[k]) + dpp<ROR8>(b3 ? p[k] : p[k + 2]);
    const int slot = 8 * kap + rho;
    *(f32x2*)(redG + w * 128 + 2 * (16 * (slot & 3) + (slot >> 2))) = f32x2{p[0], p[1]};
    SB();
  }
  static_for<JC, J2>(issue);
  bool ok = true;
  asm volatile("" :: "v"(code));
  if (__builtin_expect(any(stale(g0, tag) | stale(g1, tag)), 0)) {
#pragma nounroll
    for (unsigned spins = 0;;) {
      RELOAD();
      g0 = ld128(rs, WS_Q + goff, 0, AUX_SC1); g1 = ld128(rs, WS_Q + goff + 128, 0, AUX_SC1);
      if (!any(stale(g0, tag) | stale(g1, tag))) break;
      if (++spins > SPIN_CAP) { ok = false; break; }
    }
  }
  {
    const float qd = fold4(asf(g0[0]), asf(g0[2]), asf(g1[0]), asf(g1[2]));
    qs[32 * w + (lane >> 5) * 16 + 2 * kap + (r & 1)] = (unsigned short)pkbf(qd, qd);
  }
  wg_barrier();
  const u32x4 qv = *(const u32x4*)(qs + 8 * c);
  publish_pairs(redG, WS_GU, w < 3);
  ST(1);

  float sc[4], f1, f2, f4, f8;
  auto eat_k = [&](auto I) {
    constexpr int i = I;
    const u32x4 k = kv[i % RING];
    float d = dot2(k[0], qv[0], 0.f); d = dot2(k[1], qv[1], d); d = dot2(k[2], qv[2], d); d = dot2(k[3], qv[3], d);
    if constexpr (!(i & 1)) f1 = d;
    else {
      d = (b0 ? d : f1) + dpp<XOR1>(b0 ? f1 : d);
      if constexpr (!(i & 2)) f2 = d;
      else {
        d = (b1 ? d : f2) + dpp<XOR2>(b1 ? f2 : d);
        if constexpr (!(i & 4)) f4 = d;
        else {
          const float t1 = f4 + dpp<ROR12>(f4), t2 = d + dpp<ROR4>(d);
          d = b2 ? t2 : t1;
          if constexpr (!(i & 8)) f8 = d;
          else { d = (b3 ? d : f8) + dpp<ROR8>(b3 ? f8 : d); sc[i >> 4] = rbf(rbf(d) * 0.08838834764831845f); }
        }
      }
    }
    asm volatile("" : "+v"(d));
    SB();
  };
  constexpr int JK = first_slot_with(64);
  static_assert(JE < J0 && J0 + 2 <= JU && JU <= J1 && J1 <= JC && JC <= J2 && cons(JK) == 64 && LEAD < RING && LEND >= 6 && JK + 3 < JWARM && JWARM + LEAD < 128, "schedule");
  static_for<J2, JK + 1>([&](auto J) { constexpr int j = J; issue(J); static_for<cons(j - 1), cons(j)>(eat_k); });
  ST(2);
  const float m = fmaxf(maxhi(max8(fmaxf(fmaxf(sc[0], sc[1]), fmaxf(sc[2], sc[3])))), -3.0e38f);
#pragma unroll
  for (int i = 0; i < 4; ++i) sc[i] = expd(sc[i], m);
  const float l = sumhi(sum8((sc[0] + sc[1]) + (sc[2] + sc[3])));
  const float lsum = sumhi(sum8(lateg[1] == tag ? (float)(int)lateg[0] : 0.f));
  if (lane == 0) { *(f32x2*)(sml + 2 * w) = f32x2{m, l}; sml[8 + w] = lsum; }

  float acc[8] = {0.f, 0.f, 0.f, 0.f, 0.f, 0.f, 0.f, 0.f};
  auto eat_v = [&](auto I) {
    constexpr int i = I - 64;
    const u32x4 v = kv[(64 + i) % RING];
    const float p = dpp<BCAST + (i & 15)>(sc[i >> 4]);
#pragma unroll
    for (int k = 0; k < 4; ++k) { acc[2 * k] = fmaf(p, lo(v[k]), acc[2 * k]); acc[2 * k + 1] = fmaf(p, hi(v[k]), acc[2 * k + 1]); }
    PIN8(acc);
    SB();
  };
  u32x4 wt8[6], warm;
  const rsrc_t ro = mk((const void*)ka0[4], H * DH * D * 2), rd = mk((const void*)ka0[7], F * D * 2);
  const int dro = (2 * (rho & 3) + (rho >> 2)) * 1024 + kap * 16 + s * 128;
  auto tile = [&](auto T) {
    constexpr int t = T;
    if constexpr (t < 4) wt8[t] = ld128(ro, rho * 2048 + kap * 16, (h * 128 + 32 * w + 16 * (t >> 1) + (t & 1)) * 1024 + s * 128, AUX_NT);
    else if constexpr (t == 4) wt8[4] = ld128(rd, dro + (6 * h + w) * 8192, 0, AUX_NT);
    else wt8[5] = ld128(rd, w < 2 ? dro + (6 * h + w + 4) * 8192 : F * D * 2, 0, AUX_NT);
    SB();
  };
  auto step_v = [&](auto J) {
    constexpr int j = J;
    issue(J);
    if constexpr (j == JWARM) {
      warm = ld128(rs, r == 0 ? WS_ACC + me + w * 256 + c * 16 : r == 1 ? WS_Y + me + w * 256 + c * 16 : WS_END, 0, AUX_SC1);
      SB();
    }
    static_for<cons(j - 1), cons(j)>(eat_v);
  };
  static_for<JK + 1, JK + 3>(step_v);
  wg_barrier();
  float fw;
  {
    const f32x2 ml = *(const f32x2*)(sml + 2 * (lane & 3));
    float M = ml[0];
    M = fmaxf(M, dpp<XOR1>(M)); M = fmaxf(M, dpp<XOR2>(M));
    fw = expd(m, M);
    float L = ml[1] * expd(ml[0], M);
    L += dpp<XOR1>(L); L += dpp<XOR2>(L);
    __builtin_amdgcn_raw_buffer_store_b128(u32x4{asu(M), tag, asu(L), tag}, rs, tid < 8 ? WS_ML + (h * 8 + s) * 128 + tid * 16 : WS_END, 0, AUX_SC1);
    float lm = sml[8 + (lane & 3)];
    lm += dpp<XOR1>(lm); lm += dpp<XOR2>(lm);
    stretch = __builtin_amdgcn_readfirstlane((int)(fminf(fmaxf(lm - (float)GOV_FREE, 0.f) * GOV_GAIN, (float)GOV_MAX) * (2.f / (127 - JGA)) + 0.5f));
    asm volatile("" : "+v"(fw));
    SB();
  }
  ST(3);
  static_for<JK + 3, 128>(step_v);
  asm volatile("" :: "v"(warm));
  const int gu_off = WS_GU + (h * 8 + rho) * 1024 + w * 128 + kap * 16;
  const int gu_off1 = w < 2 ? gu_off + 512 : WS_END;
  const unsigned tag1 = w < 2 ? tag : 0u;
  const int ml_off = WS_ML + (h * 8 + rho) * 128 + kap * 16;
  tile(std::integral_constant<int, 5>{}); tile(std::integral_constant<int, 4>{});
  const u32x4 gx = ld128(rx, w == 0 ? lane * 16 : D * 2, 0, 0), gg = ld128(mk((const void*)ka1[0], D * 2), w == 0 ? lane * 16 : D * 2, 0, 0);
  u32x4 gu0 = ld128(rs, gu_off, 0, AUX_SC1), gu1 = ld128(rs, gu_off1, 0, AUX_SC1), mlg = ld128(rs, ml_off, 0, AUX_SC1);
  SB();
  static_for<0, 4>([&](auto T) { constexpr int t = T; tile(T); eat_v(std::integral_constant<int, cons(127) + t>{}); });
  static_for<cons(127) + 4, 128>(eat_v);
  *(f32x2*)(redA + w * 128 + 2 * lane) = fold8to2(acc) * fw;
  ST(4);
  wg_barrier();
  publish_pairs(redA, WS_ACC, true);
  ST(5);
  if (__builtin_expect(any(stale(gu0, tag) | stale(gu1, tag1) | stale(mlg, tag)), 0)) {
#pragma nounroll
    for (unsigned spins = 0;;) {
      RELOAD();
      gu0 = ld128(rs, gu_off, 0, AUX_SC1); gu1 = ld128(rs, gu_off1, 0, AUX_SC1); mlg = ld128(rs, ml_off, 0, AUX_SC1);
      if (!any(stale(gu0, tag) | stale(gu1, tag1) | stale(mlg, tag))) break;
      if (++spins > SPIN_CAP) { ok = false; break; }
    }
  }
  float wt;
  {
    const float ms = asf(mlg[0]);
    wt = expd(ms, maxhi(ms));
    wt *= __builtin_amdgcn_rcpf(sumhi(wt * asf(mlg[2])));
    const float gu = rbf(fold4(asf(gu0[0]), asf(gu0[2]), asf(gu1[0]), asf(gu1[2])));
    const int src = (rho & 3) + 16 * (rho >> 2);
    const float h0 = rbf(bperm(src, gu) * bperm(src + 4, gu)), h1 = rbf(bperm(src + 32, gu) * bperm(src + 36, gu));
    float y[8];
#pragma unroll
    for (int k = 0; k < 4; ++k) { y[2 * k] = fmaf(h1, lo(wt8[5][k]), h0 * lo(wt8[4][k])); y[2 * k + 1] = fmaf(h1, hi(wt8[5][k]), h0 * hi(wt8[4][k])); }
    redY[256 + w * 64 + lane] = fold8(y, b3);
  }
  float wf[4][8];
#pragma unroll
  for (int t = 0; t < 4; ++t) {
#pragma unroll
    for (int k = 0; k < 4; ++k) { wf[t][2 * k] = lo(wt8[t][k]); wf[t][2 * k + 1] = hi(wt8[t][k]); }
    PIN8(wf[t]);
  }
  asm volatile("" : "+v"(wt));
  SB();
  g0 = ld128(rs, WS_ACC + goff, 0, AUX_SC1); g1 = ld128(rs, WS_ACC + goff + 128, 0, AUX_SC1);
  if (__builtin_expect(any(stale(g0, tag) | stale(g1, tag)), 0)) {
#pragma nounroll
    for (unsigned spins = 0;;) {
      RELOAD();
      const bool s0 = stale(g0, tag) != 0u, s1 = stale(g1, tag) != 0u;
      const u32x4 n0 = ld128(rs, s0 ? WS_ACC + goff : WS_END, 0, AUX_SC1), n1 = ld128(rs, s1 ? WS_ACC + goff + 128 : WS_END, 0, AUX_SC1);
      if (s0) g0 = n0;
      if (s1) g1 = n1;
      if (!any(stale(g0, tag) | stale(g1, tag))) break;
      if (++spins > SPIN_CAP) { ok = false; break; }
    }
  }
  ST(6);
  {
    const float od = rbf(fold4(wt * asf(g0[0]), wt * asf(g0[2]), wt * asf(g1[0]), wt * asf(g1[2])));
    const float o0 = bperm(rho, od), o1 = bperm(rho + 16, od), o2 = bperm(rho + 32, od), o3 = bperm(rho + 48, od);
    float y[8];
#pragma unroll
    for (int k = 0; k < 8; ++k) y[k] = fmaf(o3, wf[3][k], fmaf(o2, wf[2][k], fmaf(o1, wf[1][k], o0 * wf[0][k])));
    const float yv = fold8(y, b3);
    redY[w * 64 + lane] = ok ? yv : __builtin_nanf("");
  }
  wg_barrier();
  if (w >= 2) { ST(7); STDUMP(); return; }
  {
    const float* p = redY + w * 256 + 8 * kap + rho;
    const float t = (p[0] + p[64]) + (p[128] + p[192]);
    __builtin_amdgcn_raw_buffer_store_b64(u32x2{asu(t), tag}, rs, WS_Y + me + w * 512 + lane * 8, 0, AUX_SC1);
  }
  ST(7);
  if (w == 1) { STDUMP(); return; }
  float gate = 0.f;
#pragma unroll
  for (int k = 0; k < 4; ++k) { gate += rbf(lo(gx[k]) * lo(gg[k])); gate += rbf(hi(gx[k]) * hi(gg[k])); }
  gate = rbf(sumhi(sum8(gate)));
  gate = rbf(1.f / (1.f + __expf(-gate)));
  if (lane >= 32) gate = rbf(1.f - gate);
  asm volatile("" : "+v"(gate));
  SB();
  const int y_off = WS_Y + ((lane & 31) * 8 + s) * 1024 + (lane >> 5) * 512 + h * 16;
  g0 = ld128(rs, y_off, 0, AUX_SC1);
#pragma nounroll
  for (unsigned spins = 0; any(stale(g0, tag));) {
    RELOAD();
    const bool s0 = stale(g0, tag) != 0u;
    const u32x4 n0 = ld128(rs, s0 ? y_off : WS_END, 0, AUX_SC1);
    if (s0) g0 = n0;
    if (++spins > SPIN_CAP) { ok = false; break; }
  }
  ST(8);
  {
    float v0 = sum8(asf(g0[0])), v1 = sum8(asf(g0[2]));
    v0 += dpp<ROR8>(v0); v1 += dpp<ROR8>(v1);
    float t = rbf(tr16(v0, v1));
    t = rbf(gate * t);
    t = tr32(t, t);
    auto r2 = __builtin_amdgcn_permlane16_swap(asu(t), asu(t), false, false);
    const unsigned o = ok ? pkbf(asf(r2[0]), asf(r2[1])) : 0x7fc07fc0u;
    __builtin_amdgcn_raw_buffer_store_b32(o, mk((const void*)ka1[1], D * 2), lane == 0 ? (32 * s + h) * 4 : D * 2, 0, 0);
    if (b == 0) __builtin_amdgcn_raw_buffer_store_b32(tag, rs, lane == 0 ? WS_EPOCH : WS_END, 0, AUX_SC1);
  }
  if (b == 0) { const unsigned tgt_ = (unsigned)tentry + 4000u + 8u * diag; while ((int)((unsigned)__builtin_amdgcn_s_memrealtime() - tgt_) < 0) __builtin_amdgcn_s_sleep(1); }
  asm volatile(".Lcode_to:");
}

extern "C" void kernel_launch(void* const* d_in, const int* in_sizes, int n_in, void* d_out, int out_size, void* d_ws, size_t ws_size,
                              hipStream_t stream) {
  (void)hipFuncSetAttribute((const void*)fused_decode, hipFuncAttributeMaxDynamicSharedMemorySize, LDS_BYTES);
  fused_decode<<<256, 256, LDS_BYTES, stream>>>(d_in[0], d_in[1], d_in[2], d_in[3], d_in[4], d_in[6], d_in[7], d_in[8], d_in[9], d_out, d_ws);
}
```

```cpp
#include <hip/hip_runtime.h>
#include <hip/hip_bf16.h>
#include <cstdint>
#include <utility>

constexpr int TAU = 3686;
constexpr int TOFF = 60;
constexpr int LEAD = 16;
constexpr int LEND = 10;
constexpr int CATCH = 4;
constexpr int JE = 4, JT = 12;
constexpr int J0 = 16;
constexpr int JU = 22;
constexpr int J1 = 32;
constexpr int JC = 38;
constexpr int J2 = 44;
constexpr int JL = 48, JG = 62, JGA = 83;
constexpr int JWARM = 108;
constexpr int RING = J2 + 1;
constexpr int OSHIFT = 10, GOV_THR = 40, GOV_FREE = 1500, GOV_MAX = 140;
constexpr float GOV_GAIN = 0.025f;

template <int A, int... I, class Fn> __device__ __forceinline__ void static_for_impl(std::integer_sequence<int, I...>, Fn&& f) { (f(std::integral_constant<int, A + I>{}), ...); }
template <int A, int B, class Fn> __device__ __forceinline__ void static_for(Fn&& f) { if constexpr (B > A) static_for_impl<A>(std::make_integer_sequence<int, B - A>{}, f); }
constexpr int cmin(int a, int b) { return a < b ? a : b; }
constexpr int cmax(int a, int b) { return a > b ? a : b; }
constexpr int cons(int j) { return j < J2 ? 0 : cmin(CATCH * (j - J2 + 1), cmax(cmax(j + 1 - LEAD, 128 - LEND - 2 * (127 - j)), 0)); }
constexpr int first_slot_with(int n) { int j = 0; while (cons(j) < n) ++j; return j; }
constexpr int slot_ticks(int j) { return ((j + (j >= JU ? 3 : 0)) * TAU) >> 8; }

typedef unsigned u32x4 __attribute__((ext_vector_type(4)));
typedef unsigned u32x2 __attribute__((ext_vector_type(2)));
typedef unsigned long long u64x8 __attribute__((ext_vector_type(8)));
typedef unsigned long long u64x2 __attribute__((ext_vector_type(2)));
typedef float f32x2 __attribute__((ext_vector_type(2)));
typedef __bf16 bf16x2 __attribute__((ext_vector_type(2)));
typedef __amdgpu_buffer_rsrc_t rsrc_t;

constexpr int D = 512, H = 32, SK = 8192, DH = 128, F = 1376;
constexpr int AUX_NT = 2, AUX_SC1 = 16, RSRC_W3 = 0x00020000;
constexpr int WS_EPOCH = 0;
constexpr int WS_T0 = 1024;
constexpr int WS_LATE = 2048;
constexpr int WS_ML = 4096;
constexpr int WS_Q = 65536;
constexpr int WS_GU = WS_Q + 256 * 1024;
constexpr int WS_ACC = WS_GU + 256 * 1024;
constexpr int WS_Y = WS_ACC + 256 * 1024;
constexpr int WS_END = WS_Y + 256 * 1024;
constexpr unsigned SPIN_CAP = 1u << 18;
constexpr int LDS_BYTES = 96 * 1024;

__device__ __forceinline__ float lo(unsigned u) { return __builtin_bit_cast(float, u << 16); }
__device__ __forceinline__ float hi(unsigned u) { return __builtin_bit_cast(float, u & 0xffff0000u); }
__device__ __forceinline__ float asf(unsigned u) { return __builtin_bit_cast(float, u); }
__device__ __forceinline__ unsigned asu(float f) { return __builtin_bit_cast(unsigned, f); }
__device__ __forceinline__ unsigned pkbf(float a, float b) {
  bf16x2 v = {(__bf16)a, (__bf16)b};
  return __builtin_bit_cast(unsigned, v);
}
__device__ __forceinline__ float rbf(float f) { return lo(pkbf(f, f)); }
__device__ __forceinline__ float expd(float a, float b) { return __builtin_amdgcn_exp2f((a - b) * 1.4426950408889634f); }
__device__ __forceinline__ float dot2(unsigned k, unsigned q, float acc) {
  return __builtin_amdgcn_fdot2_f32_bf16(__builtin_bit_cast(bf16x2, k), __builtin_bit_cast(bf16x2, q), acc, false);
}
template <int CTRL> __device__ __forceinline__ int dppi(int x) { return __builtin_amdgcn_mov_dpp(x, CTRL, 0xf, 0xf, true); }
template <int CTRL> __device__ __forceinline__ float dpp(float x) { return __builtin_bit_cast(float, dppi<CTRL>(__builtin_bit_cast(int, x))); }
constexpr int XOR1 = 0xB1, XOR2 = 0x4E, XOR7 = 0x141, ROR8 = 0x128, ROR4 = 0x124, ROR12 = 0x12C, BCAST = 0x150;
__device__ __forceinline__ float sum8(float x) { x += dpp<XOR1>(x); x += dpp<XOR2>(x); x += dpp<XOR7>(x); return x; }
__device__ __forceinline__ float max8(float x) { x = fmaxf(x, dpp<XOR1>(x)); x = fmaxf(x, dpp<XOR2>(x)); x = fmaxf(x, dpp<XOR7>(x)); return x; }
__device__ __forceinline__ float tr32(float a, float b) {
  auto r = __builtin_amdgcn_permlane32_swap(asu(a), asu(b), false, false);
  return asf(r[0]) + asf(r[1]);
}
__device__ __forceinline__ float tr16(float a, float b) {
  auto r = __builtin_amdgcn_permlane16_swap(asu(a), asu(b), false, false);
  return asf(r[0]) + asf(r[1]);
}
__device__ __forceinline__ float maxhi(float a) {
  a = fmaxf(a, dpp<ROR8>(a));
  auto r = __builtin_amdgcn_permlane16_swap(asu(a), asu(a), false, false);
  a = fmaxf(asf(r[0]), asf(r[1]));
  auto t = __builtin_amdgcn_permlane32_swap(asu(a), asu(a), false, false);
  return fmaxf(asf(t[0]), asf(t[1]));
}
__device__ __forceinline__ float sumhi(float x) { x += dpp<ROR8>(x); x = tr16(x, x); return tr32(x, x); }
__device__ __forceinline__ float fold4(float a0, float a1, float b0, float b1) { float x = tr16(tr32(a0, b0), tr32(a1, b1)); return x + dpp<ROR8>(x); }
__device__ __forceinline__ float fold8(const float (&y)[8], bool b3) {
  const float t0 = tr32(y[0], y[4]), t1 = tr32(y[1], y[5]), t2 = tr32(y[2], y[6]), t3 = tr32(y[3], y[7]);
  const float u0 = tr16(t0, t2), u1 = tr16(t1, t3);
  return (b3 ? u1 : u0) + dpp<ROR8>(b3 ? u0 : u1);
}
__device__ __forceinline__ f32x2 fold8to2(const float (&y)[8]) {
  const float t0 = tr32(y[0], y[4]), t1 = tr32(y[1], y[5]), t2 = tr32(y[2], y[6]), t3 = tr32(y[3], y[7]);
  return f32x2{tr16(t0, t2), tr16(t1, t3)};
}
__device__ __forceinline__ float bperm(int src_lane, float x) { return asf(__builtin_amdgcn_ds_bpermute(src_lane * 4, asu(x))); }
__device__ __forceinline__ void wg_barrier() {
  __builtin_amdgcn_fence(__ATOMIC_RELEASE, "workgroup");
  __builtin_amdgcn_s_barrier();
  __builtin_amdgcn_fence(__ATOMIC_ACQUIRE, "workgroup");
}
__device__ __forceinline__ rsrc_t mk(const void* p, int bytes) { return __builtin_amdgcn_make_buffer_rsrc((void*)p, 0, bytes, RSRC_W3); }
#define ld128(r, voff, soff, aux) __builtin_amdgcn_raw_buffer_load_b128((r), (voff), (soff), (aux))
__device__ __forceinline__ unsigned stale(u32x4 g, unsigned tag) { return (g[1] ^ tag) | (g[3] ^ tag); }
__device__ __forceinline__ bool any(unsigned v) { return __builtin_amdgcn_ballot_w64(v != 0u) != 0ull; }
#define SB() __builtin_amdgcn_sched_barrier(0)
#define PIN8(a) asm volatile("" : "+v"(a[0]), "+v"(a[1]), "+v"(a[2]), "+v"(a[3]), "+v"(a[4]), "+v"(a[5]), "+v"(a[6]), "+v"(a[7]))
#define RELOAD() asm volatile("" ::: "memory")

__global__ void __launch_bounds__(256, 1)
fused_decode(const void* a_x, const void* a_k, const void* a_v, const void* a_wq, const void* a_wo, const void* a_wg,
             const void* a_wu, const void* a_wd, const void* a_gw, void* a_out, void* a_ws) {
  u64x8 ka0; u64x2 ka1; unsigned long long ka2, tentry, pc; unsigned code, ctmp;
  asm volatile(
      "s_load_dwordx16 %[a0], %[ka], 0x0\n\t"
      "s_memrealtime %[t]\n\t"
      "s_getpc_b64 %[pc]\n"
      ".Lcode_from:\n\t"
      "s_lshr_b32 %[c], %[wg], 3\n\t"
      "s_lshl_b32 %[c], %[c], 10\n\t"
      "v_lshl_add_u32 %[v], %[tid], 2, %[c]\n\t"
      "v_min_u32 %[v], .Lcode_to-.Lcode_from-4, %[v]\n\t"
      "global_load_dword %[v], %[v], %[pc]\n\t"
      "s_load_dwordx4 %[a1], %[ka], 0x40\n\t"
      "s_load_dwordx2 %[a2], %[ka], 0x50"
      : [a0] "=&s"(ka0), [a1] "=&s"(ka1), [a2] "=&s"(ka2), [t] "=&s"(tentry), [pc] "=&s"(pc), [c] "=&s"(ctmp), [v] "=&v"(code)
      : [ka] "s"(__builtin_amdgcn_kernarg_segment_ptr()), [wg] "s"(blockIdx.x), [tid] "v"(threadIdx.x)
      : "scc", "memory");
  extern __shared__ __attribute__((aligned(16))) float lds[];
  float* const redQ = lds;
  float* const redG = lds + 512;
  float* const redA = lds + 1024;
  unsigned short* const qs = (unsigned short*)(lds + 1536);
  float* const sml = lds + 1600;
  float* const redY = lds + 1664;

  const int b = blockIdx.x, h = (b & 7) * 4 + (b >> 6), s = (b >> 3) & 7;
  const int me = (h * 8 + s) * 1024;
  const int tid = threadIdx.x, lane = tid & 63, w = __builtin_amdgcn_readfirstlane(tid >> 6);
  const int r = lane >> 4, c = lane & 15, rho = lane >> 3, kap = lane & 7;
  const bool b0 = lane & 1, b1 = lane & 2, b2 = lane & 4, b3 = lane & 8;
  const int row0 = 64 * s + 16 * w;
  int wq_v = r * 4 * 8192 + c * 16, wq_s = row0 * 8192 + h * 256;
  asm volatile("s_waitcnt lgkmcnt(0)" : "+s"(ka0), "+s"(ka1), "+s"(ka2), "+s"(tentry), "+v"(wq_v), "+s"(wq_s) :: "memory");
  unsigned char* const ws = (unsigned char*)ka2;
  const size_t kvb = (size_t)(h * SK + s * 1024) * 256;
  const rsrc_t rs = mk(ws, WS_END), rx = mk((const void*)ka0[0], D * 2);
  const rsrc_t rk = mk((const char*)ka0[1] + kvb, 1024 * 256), rv = mk((const char*)ka0[2] + kvb, 1024 * 256);
  unsigned tstart = (unsigned)tentry + TOFF;

  u32x4 kv[RING];
  unsigned stretch = 0, tag = 0;
  u32x2 lateg;
  auto issue = [&](auto J) {
    constexpr int j = J;
    if constexpr (j % 2 == 0) {
      if constexpr (j > JGA) tstart += stretch;
      int late;
      do { late = (int)((unsigned)__builtin_amdgcn_s_memrealtime() - (tstart + slot_ticks(j))); } while (late < 0);
      if constexpr (j == JL) {
        if (late > GOV_THR && w == 0) __builtin_amdgcn_raw_buffer_store_b64(u32x2{(unsigned)late, tag}, rs, lane == 0 ? WS_LATE + b * 8 : WS_END, 0, AUX_SC1);
      }
    }
    if constexpr (j == JG) lateg = __builtin_amdgcn_raw_buffer_load_b64(rs, WS_LATE + tid * 8, 0, AUX_SC1);
    if constexpr (j < 64) kv[j % RING] = ld128(rk, tid * 16, j * 4096, AUX_NT); else kv[j % RING] = ld128(rv, tid * 16, (j - 64) * 4096, AUX_NT);
    SB();
  };
  const unsigned epoch = __builtin_amdgcn_raw_buffer_load_b32(rs, WS_EPOCH, 0, AUX_SC1);
  u32x4 wqv[4];
  {
    const rsrc_t rq = mk((const void*)ka0[3], D * H * DH * 2);
#pragma unroll
    for (int j = 0; j < 4; ++j) wqv[j] = ld128(rq, wq_v, wq_s + j * 8192, AUX_NT);
  }
  const u32x2 xq = __builtin_amdgcn_raw_buffer_load_b64(rx, 8 * r, row0 * 2, 0);
  SB();
  static_for<0, JE>(issue);
  tag = epoch * 0x9E3779B1u + 0x7F4A7C15u;
  __builtin_amdgcn_raw_buffer_store_b128(u32x4{(unsigned)tentry, tag, (unsigned)tentry, tag}, rs, (b < 8 && tid < 8) ? WS_T0 + b * 128 + lane * 16 : WS_END, 0, AUX_SC1);
  SB();
  static_for<JE, JT>(issue);
  const u32x2 t0g = __builtin_amdgcn_raw_buffer_load_b64(rs, WS_T0 + kap * 128, 0, AUX_SC1);
  SB();
  static_for<JT, J0>(issue);
  {
    float qa[8];
#pragma unroll
    for (int j = 0; j < 4; ++j) {
      const float xv = (j & 1) ? hi(xq[j >> 1]) : lo(xq[j >> 1]);
#pragma unroll
      for (int k = 0; k < 4; ++k) {
        qa[2 * k] = j ? fmaf(xv, lo(wqv[j][k]), qa[2 * k]) : xv * lo(wqv[j][k]);
        qa[2 * k + 1] = j ? fmaf(xv, hi(wqv[j][k]), qa[2 * k + 1]) : xv * hi(wqv[j][k]);
      }
    }
    *(f32x2*)(redQ + w * 128 + 2 * lane) = fold8to2(qa);
  }
  SB();
  static_for<J0, J0 + 2>(issue);
  const int pub_lds = r * 128 + 2 * (16 * (c & 3) + 4 * w + (c >> 2)), pub_off = lane < 16 ? me + (16 * w + c) * 16 : WS_END;
  auto publish_pairs = [&](const float* red, int base, bool on) {
    const f32x2 a = *(const f32x2*)(red + pub_lds);
    float t = tr32(a[0], a[1]);
    t = tr16(t, t);
    auto r2 = __builtin_amdgcn_permlane32_swap(asu(t), asu(t), false, false);
    __builtin_amdgcn_raw_buffer_store_b128(u32x4{r2[0], tag, r2[1], tag}, rs, on ? pub_off : WS_END, base, AUX_SC1);
    SB();
  };
  wg_barrier();
  publish_pairs(redQ, WS_Q, true);
  static_for<J0 + 2, JT + 10>(issue);
  {
    const bool have = t0g[1] == tag;
    const float dsum = sum8(have ? (float)(int)(t0g[0] - (unsigned)tentry) : 0.f), cnt = sum8(have ? 1.f : 0.f);
    const float shift = fminf(fmaxf(dsum / fmaxf(cnt, 1.f) - (cnt > 0.f ? (float)OSHIFT : 0.f), -300.f), 100.f);
    tstart += __builtin_amdgcn_readfirstlane((int)shift);
    SB();
  }
  static_for<JT + 10, JU>(issue);
  u32x4 gv[2], uv[2];
  {
    const rsrc_t rg = mk((const void*)ka0[5], D * F * 2), ru = mk((const void*)ka0[6], D * F * 2);
    const int chunk = 6 * h + kap;
    const int go = (kap < 6 && chunk < F / 8) ? rho * (F * 2) + chunk * 16 : D * F * 2;
#pragma unroll
    for (int t = 0; t < 2; ++t) { gv[t] = ld128(rg, go, (row0 + 8 * t) * (F * 2), AUX_NT); uv[t] = ld128(ru, go, (row0 + 8 * t) * (F * 2), AUX_NT); }
  }
  const unsigned short xg0 = __builtin_amdgcn_raw_buffer_load_b16(rx, 2 * rho, row0 * 2, 0);
  const unsigned short xg1 = __builtin_amdgcn_raw_buffer_load_b16(rx, 2 * rho + 16, row0 * 2, 0);
  SB();
  static_for<JU, J1>(issue);
  const int goff = (h * 8 + rho) * 1024 + w * 256 + kap * 16;
  u32x4 g0 = ld128(rs, WS_Q + goff, 0, AUX_SC1), g1 = ld128(rs, WS_Q + goff + 128, 0, AUX_SC1);
  SB();
  static_for<J1, JC>(issue);
  {
    float p[16];
    const float x0 = lo(xg0), x1 = lo(xg1);
#pragma unroll
    for (int k = 0; k < 4; ++k) {
      p[2 * k] = fmaf(x1, lo(gv[1][k]), x0 * lo(gv[0][k])); p[2 * k + 1] = fmaf(x1, hi(gv[1][k]), x0 * hi(gv[0][k]));
      p[8 + 2 * k] = fmaf(x1, lo(uv[1][k]), x0 * lo(uv[0][k])); p[9 + 2 * k] = fmaf(x1, hi(uv[1][k]), x0 * hi(uv[0][k]));
    }
#pragma unroll
    for (int k = 0; k < 8; ++k) p[k] = tr32(p[k], p[k + 8]);
#pragma unroll
    for (int k = 0; k < 4; ++k) p[k] = tr16(p[k], p[k + 4]);
#pragma unroll
    for (int k = 0; k < 2; ++k) p[k] = (b3 ? p[k + 2] : p[k]) + dpp<ROR8>(b3 ? p[k] : p[k + 2]);
    const int slot = 8 * kap + rho;
    *(f32x2*)(redG + w * 128 + 2 * (16 * (slot & 3) + (slot >> 2))) = f32x2{p[0], p[1]};
    SB();
  }
  static_for<JC, J2>(issue);
  bool ok = true;
  asm volatile("" :: "v"(code));
  if (__builtin_expect(any(stale(g0, tag) | stale(g1, tag)), 0)) {
#pragma nounroll
    for (unsigned spins = 0;;) {
      RELOAD();
      g0 = ld128(rs, WS_Q + goff, 0, AUX_SC1); g1 = ld128(rs, WS_Q + goff + 128, 0, AUX_SC1);
      if (!any(stale(g0, tag) | stale(g1, tag))) break;
      if (++spins > SPIN_CAP) { ok = false; break; }
    }
  }
  {
    const float qd = fold4(asf(g0[0]), asf(g0[2]), asf(g1[0]), asf(g1[2]));
    qs[32 * w + (lane >> 5) * 16 + 2 * kap + (r & 1)] = (unsigned short)pkbf(qd, qd);
  }
  wg_barrier();
  const u32x4 qv = *(const u32x4*)(qs + 8 * c);
  publish_pairs(redG, WS_GU, w < 3);

  float sc[4], f1, f2, f4, f8;
  auto eat_k = [&](auto I) {
    constexpr int i = I;
    const u32x4 k = kv[i % RING];
    float d = dot2(k[0], qv[0], 0.f); d = dot2(k[1], qv[1], d); d = dot2(k[2], qv[2], d); d = dot2(k[3], qv[3], d);
    if constexpr (!(i & 1)) f1 = d;
    else {
      d = (b0 ? d : f1) + dpp<XOR1>(b0 ? f1 : d);
      if constexpr (!(i & 2)) f2 = d;
      else {
        d = (b1 ? d : f2) + dpp<XOR2>(b1 ? f2 : d);
        if constexpr (!(i & 4)) f4 = d;
        else {
          const float t1 = f4 + dpp<ROR12>(f4), t2 = d + dpp<ROR4>(d);
          d = b2 ? t2 : t1;
          if constexpr (!(i & 8)) f8 = d;
          else { d = (b3 ? d : f8) + dpp<ROR8>(b3 ? f8 : d); sc[i >> 4] = rbf(rbf(d) * 0.08838834764831845f); }
        }
      }
    }
    asm volatile("" : "+v"(d));
    SB();
  };
  constexpr int JK = first_slot_with(64);
  static_assert(JE < JT && JT <= J0 && J0 + 2 <= JT + 10 && JT + 10 <= JU && JU <= J1 && J1 <= JC && JC <= J2 && cons(JK) == 64 && LEAD < RING && LEND >= 6 && JK + 3 < JWARM && JWARM + LEAD < 128, "schedule");
  static_for<J2, JK + 1>([&](auto J) { constexpr int j = J; issue(J); static_for<cons(j - 1), cons(j)>(eat_k); });
  const float m = fmaxf(maxhi(max8(fmaxf(fmaxf(sc[0], sc[1]), fmaxf(sc[2], sc[3])))), -3.0e38f);
#pragma unroll
  for (int i = 0; i < 4; ++i) sc[i] = expd(sc[i], m);
  const float l = sumhi(sum8((sc[0] + sc[1]) + (sc[2] + sc[3])));
  const float lsum = sumhi(sum8(lateg[1] == tag ? (float)(int)lateg[0] : 0.f));
  if (lane == 0) { *(f32x2*)(sml + 2 * w) = f32x2{m, l}; sml[8 + w] = lsum; }

  float acc[8] = {0.f, 0.f, 0.f, 0.f, 0.f, 0.f, 0.f, 0.f};
  auto eat_v = [&](auto I) {
    constexpr int i = I - 64;
    const u32x4 v = kv[(64 + i) % RING];
    const float p = dpp<BCAST + (i & 15)>(sc[i >> 4]);
#pragma unroll
    for (int k = 0; k < 4; ++k) { acc[2 * k] = fmaf(p, lo(v[k]), acc[2 * k]); acc[2 * k + 1] = fmaf(p, hi(v[k]), acc[2 * k + 1]); }
    PIN8(acc);
    SB();
  };
  u32x4 wt8[6], warm;
  const rsrc_t ro = mk((const void*)ka0[4], H * DH * D * 2), rd = mk((const void*)ka0[7], F * D * 2);
  const int dro = (2 * (rho & 3) + (rho >> 2)) * 1024 + kap * 16 + s * 128;
  auto tile = [&](auto T) {
    constexpr int t = T;
    if constexpr (t < 4) wt8[t] = ld128(ro, rho * 2048 + kap * 16, (h * 128 + 32 * w + 16 * (t >> 1) + (t & 1)) * 1024 + s * 128, AUX_NT);
    else if constexpr (t == 4) wt8[4] = ld128(rd, dro + (6 * h + w) * 8192, 0, AUX_NT);
    else wt8[5] = ld128(rd, w < 2 ? dro + (6 * h + w + 4) * 8192 : F * D * 2, 0, AUX_NT);
    SB();
  };
  auto step_v = [&](auto J) {
    constexpr int j = J;
    issue(J);
    if constexpr (j == JWARM) {
      warm = ld128(rs, r == 0 ? WS_ACC + me + w * 256 + c * 16 : r == 1 ? WS_Y + me + w * 256 + c * 16 : WS_END, 0, AUX_SC1);
      SB();
    }
    static_for<cons(j - 1), cons(j)>(eat_v);
  };
  static_for<JK + 1, JK + 3>(step_v);
  wg_barrier();
  float fw;
  {
    const f32x2 ml = *(const f32x2*)(sml + 2 * (lane & 3));
    float M = ml[0];
    M = fmaxf(M, dpp<XOR1>(M)); M = fmaxf(M, dpp<XOR2>(M));
    fw = expd(m, M);
    float L = ml[1] * expd(ml[0], M);
    L += dpp<XOR1>(L); L += dpp<XOR2>(L);
    __builtin_amdgcn_raw_buffer_store_b128(u32x4{asu(M), tag, asu(L), tag}, rs, tid < 8 ? WS_ML + (h * 8 + s) * 128 + tid * 16 : WS_END, 0, AUX_SC1);
    float lm = sml[8 + (lane & 3)];
    lm += dpp<XOR1>(lm); lm += dpp<XOR2>(lm);
    stretch = __builtin_amdgcn_readfirstlane((int)(fminf(fmaxf(lm - (float)GOV_FREE, 0.f) * GOV_GAIN, (float)GOV_MAX) * (2.f / (127 - JGA)) + 0.5f));
    asm volatile("" : "+v"(fw));
    SB();
  }
  static_for<JK + 3, 128>(step_v);
  asm volatile("" :: "v"(warm));
  const int gu_off = WS_GU + (h * 8 + rho) * 1024 + w * 128 + kap * 16;
  const int gu_off1 = w < 2 ? gu_off + 512 : WS_END;
  const unsigned tag1 = w < 2 ? tag : 0u;
  const int ml_off = WS_ML + (h * 8 + rho) * 128 + kap * 16;
  static_for<cons(127), cons(127) + 4>(eat_v);
  tile(std::integral_constant<int, 5>{}); tile(std::integral_constant<int, 4>{});
  const u32x4 gx = ld128(rx, w == 0 ? lane * 16 : D * 2, 0, 0), gg = ld128(mk((const void*)ka1[0], D * 2), w == 0 ? lane * 16 : D * 2, 0, 0);
  SB();
  u32x4 gu0 = ld128(rs, gu_off, 0, AUX_SC1), gu1 = ld128(rs, gu_off1, 0, AUX_SC1), mlg = ld128(rs, ml_off, 0, AUX_SC1);
  SB();
  static_for<0, 4>(tile);
  static_for<cons(127) + 4, 128>(eat_v);
  *(f32x2*)(redA + w * 128 + 2 * lane) = fold8to2(acc) * fw;
  wg_barrier();
  publish_pairs(redA, WS_ACC, true);
  if (__builtin_expect(any(stale(gu0, tag) | stale(gu1, tag1) | stale(mlg, tag)), 0)) {
#pragma nounroll
    for (unsigned spins = 0;;) {
      RELOAD();
      gu0 = ld128(rs, gu_off, 0, AUX_SC1); gu1 = ld128(rs, gu_off1, 0, AUX_SC1); mlg = ld128(rs, ml_off, 0, AUX_SC1);
      if (!any(stale(gu0, tag) | stale(gu1, tag1) | stale(mlg, tag))) break;
      if (++spins > SPIN_CAP) { ok = false; break; }
    }
  }
  float wt;
  {
    const float ms = asf(mlg[0]);
    wt = expd(ms, maxhi(ms));
    wt *= __builtin_amdgcn_rcpf(sumhi(wt * asf(mlg[2])));
    const float gu = rbf(fold4(asf(gu0[0]), asf(gu0[2]), asf(gu1[0]), asf(gu1[2])));
    const int src = (rho & 3) + 16 * (rho >> 2);
    const float h0 = rbf(bperm(src, gu) * bperm(src + 4, gu)), h1 = rbf(bperm(src + 32, gu) * bperm(src + 36, gu));
    float y[8];
#pragma unroll
    for (int k = 0; k < 4; ++k) { y[2 * k] = fmaf(h1, lo(wt8[5][k]), h0 * lo(wt8[4][k])); y[2 * k + 1] = fmaf(h1, hi(wt8[5][k]), h0 * hi(wt8[4][k])); }
    redY[256 + w * 64 + lane] = fold8(y, b3);
  }
  float wf[4][8];
#pragma unroll
  for (int t = 0; t < 4; ++t) {
#pragma unroll
    for (int k = 0; k < 4; ++k) { wf[t][2 * k] = lo(wt8[t][k]); wf[t][2 * k + 1] = hi(wt8[t][k]); }
    PIN8(wf[t]);
  }
  asm volatile("" : "+v"(wt));
  SB();
  g0 = ld128(rs, WS_ACC + goff, 0, AUX_SC1); g1 = ld128(rs, WS_ACC + goff + 128, 0, AUX_SC1);
  if (__builtin_expect(any(stale(g0, tag) | stale(g1, tag)), 0)) {
#pragma nounroll
    for (unsigned spins = 0;;) {
      RELOAD();
      const bool s0 = stale(g0, tag) != 0u, s1 = stale(g1, tag) != 0u;
      const u32x4 n0 = ld128(rs, s0 ? WS_ACC + goff : WS_END, 0, AUX_SC1), n1 = ld128(rs, s1 ? WS_ACC + goff + 128 : WS_END, 0, AUX_SC1);
      if (s0) g0 = n0;
      if (s1) g1 = n1;
      if (!any(stale(g0, tag) | stale(g1, tag))) break;
      if (++spins > SPIN_CAP) { ok = false; break; }
    }
  }
  {
    const float od = rbf(fold4(wt * asf(g0[0]), wt * asf(g0[2]), wt * asf(g1[0]), wt * asf(g1[2])));
    const float o0 = bperm(rho, od), o1 = bperm(rho + 16, od), o2 = bperm(rho + 32, od), o3 = bperm(rho + 48, od);
    float y[8];
#pragma unroll
    for (int k = 0; k < 8; ++k) y[k] = fmaf(o3, wf[3][k], fmaf(o2, wf[2][k], fmaf(o1, wf[1][k], o0 * wf[0][k])));
    const float yv = fold8(y, b3);
    redY[w * 64 + lane] = ok ? yv : __builtin_nanf("");
  }
  wg_barrier();
  if (w >= 2) return;
  {
    const float* p = redY + w * 256 + 8 * kap + rho;
    const float t = (p[0] + p[64]) + (p[128] + p[192]);
    __builtin_amdgcn_raw_buffer_store_b64(u32x2{asu(t), tag}, rs, WS_Y + me + w * 512 + lane * 8, 0, AUX_SC1);
  }
  if (w == 1) return;
  float gate = 0.f;
#pragma unroll
  for (int k = 0; k < 4; ++k) { gate += rbf(lo(gx[k]) * lo(gg[k])); gate += rbf(hi(gx[k]) * hi(gg[k])); }
  gate = rbf(sumhi(sum8(gate)));
  gate = rbf(1.f / (1.f + __expf(-gate)));
  if (lane >= 32) gate = rbf(1.f - gate);
  asm volatile("" : "+v"(gate));
  SB();
  const int y_off = WS_Y + ((lane & 31) * 8 + s) * 1024 + (lane >> 5) * 512 + h * 16;
  __builtin_amdgcn_s_sleep(12);
  g0 = ld128(rs, y_off, 0, AUX_SC1);
#pragma nounroll
  for (unsigned spins = 0; any(stale(g0, tag));) {
    RELOAD();
    const bool s0 = stale(g0, tag) != 0u;
    const u32x4 n0 = ld128(rs, s0 ? y_off : WS_END, 0, AUX_SC1);
    if (s0) g0 = n0;
    if (++spins > SPIN_CAP) { ok = false; break; }
  }
  {
    float v0 = sum8(asf(g0[0])), v1 = sum8(asf(g0[2]));
    v0 += dpp<ROR8>(v0); v1 += dpp<ROR8>(v1);
    float t = rbf(tr16(v0, v1));
    t = rbf(gate * t);
    t = tr32(t, t);
    auto r2 = __builtin_amdgcn_permlane16_swap(asu(t), asu(t), false, false);
    const unsigned o = ok ? pkbf(asf(r2[0]), asf(r2[1])) : 0x7fc07fc0u;
    __builtin_amdgcn_raw_buffer_store_b32(o, mk((const void*)ka1[1], D * 2), lane == 0 ? (32 * s + h) * 4 : D * 2, 0, 0);
    if (b == 0) __builtin_amdgcn_raw_buffer_store_b32(tag, rs, lane == 0 ? WS_EPOCH : WS_END, 0, AUX_SC1);
  }
  asm volatile(".Lcode_to:");
}

extern "C" void kernel_launch(void* const* d_in, const int* in_sizes, int n_in, void* d_out, int out_size, void* d_ws, size_t ws_size,
                              hipStream_t stream) {
  (void)hipFuncSetAttribute((const void*)fused_decode, hipFuncAttributeMaxDynamicSharedMemorySize, LDS_BYTES);
  fused_decode<<<256, 256, LDS_BYTES, stream>>>(d_in[0], d_in[1], d_in[2], d_in[3], d_in[4], d_in[6], d_in[7], d_in[8], d_in[9], d_out, d_ws);
}
```

```cpp
#define DJ 126
#define DEXPR dlate
#include <hip/hip_runtime.h>
#include <hip/hip_bf16.h>
#include <cstdint>
#include <utility>

constexpr int TAU = 3686;
constexpr int TOFF = 60;
constexpr int LEAD = 16;
constexpr int LEND = 10;
constexpr int CATCH = 4;
constexpr int JE = 4, JT = 12;
constexpr int J0 = 16;
constexpr int JU = 22;
constexpr int J1 = 32;
constexpr int JC = 38;
constexpr int J2 = 44;
constexpr int JL = 48, JG = 62, JGA = 83;
constexpr int JWARM = 108;
constexpr int RING = J2 + 1;
constexpr int OSHIFT = 10, GOV_THR = 40, GOV_FREE = 1500, GOV_MAX = 140;
constexpr float GOV_GAIN = 0.025f;

template <int A, int... I, class Fn> __device__ __forceinline__ void static_for_impl(std::integer_sequence<int, I...>, Fn&& f) { (f(std::integral_constant<int, A + I>{}), ...); }
template <int A, int B, class Fn> __device__ __forceinline__ void static_for(Fn&& f) { if constexpr (B > A) static_for_impl<A>(std::make_integer_sequence<int, B - A>{}, f); }
constexpr int cmin(int a, int b) { return a < b ? a : b; }
constexpr int cmax(int a, int b) { return a > b ? a : b; }
constexpr int cons(int j) { return j < J2 ? 0 : cmin(CATCH * (j - J2 + 1), cmax(cmax(j + 1 - LEAD, 128 - LEND - 2 * (127 - j)), 0)); }
constexpr int first_slot_with(int n) { int j = 0; while (cons(j) < n) ++j; return j; }
constexpr int slot_ticks(int j) { return ((j + (j >= JU ? 3 : 0)) * TAU) >> 8; }

typedef unsigned u32x4 __attribute__((ext_vector_type(4)));
typedef unsigned u32x2 __attribute__((ext_vector_type(2)));
typedef unsigned long long u64x8 __attribute__((ext_vector_type(8)));
typedef unsigned long long u64x2 __attribute__((ext_vector_type(2)));
typedef float f32x2 __attribute__((ext_vector_type(2)));
typedef __bf16 bf16x2 __attribute__((ext_vector_type(2)));
typedef __amdgpu_buffer_rsrc_t rsrc_t;

constexpr int D = 512, H = 32, SK = 8192, DH = 128, F = 1376;
constexpr int AUX_NT = 2, AUX_SC1 = 16, RSRC_W3 = 0x00020000;
constexpr int WS_EPOCH = 0;
constexpr int WS_T0 = 1024;
constexpr int WS_LATE = 2048;
constexpr int WS_ML = 4096;
constexpr int WS_Q = 65536;
constexpr int WS_GU = WS_Q + 256 * 1024;
constexpr int WS_ACC = WS_GU + 256 * 1024;
constexpr int WS_Y = WS_ACC + 256 * 1024;
constexpr int WS_END = WS_Y + 256 * 1024;
constexpr unsigned SPIN_CAP = 1u << 18;
constexpr int LDS_BYTES = 96 * 1024;

__device__ __forceinline__ float lo(unsigned u) { return __builtin_bit_cast(float, u << 16); }
__device__ __forceinline__ float hi(unsigned u) { return __builtin_bit_cast(float, u & 0xffff0000u); }
__device__ __forceinline__ float asf(unsigned u) { return __builtin_bit_cast(float, u); }
__device__ __forceinline__ unsigned asu(float f) { return __builtin_bit_cast(unsigned, f); }
__device__ __forceinline__ unsigned pkbf(float a, float b) {
  bf16x2 v = {(__bf16)a, (__bf16)b};
  return __builtin_bit_cast(unsigned, v);
}
__device__ __forceinline__ float rbf(float f) { return lo(pkbf(f, f)); }
__device__ __forceinline__ float expd(float a, float b) { return __builtin_amdgcn_exp2f((a - b) * 1.4426950408889634f); }
__device__ __forceinline__ float dot2(unsigned k, unsigned q, float acc) {
  return __builtin_amdgcn_fdot2_f32_bf16(__builtin_bit_cast(bf16x2, k), __builtin_bit_cast(bf16x2, q), acc, false);
}
template <int CTRL> __device__ __forceinline__ int dppi(int x) { return __builtin_amdgcn_mov_dpp(x, CTRL, 0xf, 0xf, true); }
template <int CTRL> __device__ __forceinline__ float dpp(float x) { return __builtin_bit_cast(float, dppi<CTRL>(__builtin_bit_cast(int, x))); }
constexpr int XOR1 = 0xB1, XOR2 = 0x4E, XOR7 = 0x141, ROR8 = 0x128, ROR4 = 0x124, ROR12 = 0x12C, BCAST = 0x150;
__device__ __forceinline__ float sum8(float x) { x += dpp<XOR1>(x); x += dpp<XOR2>(x); x += dpp<XOR7>(x); return x; }
__device__ __forceinline__ float max8(float x) { x = fmaxf(x, dpp<XOR1>(x)); x = fmaxf(x, dpp<XOR2>(x)); x = fmaxf(x, dpp<XOR7>(x)); return x; }
__device__ __forceinline__ float tr32(float a, float b) {
  auto r = __builtin_amdgcn_permlane32_swap(asu(a), asu(b), false, false);
  return asf(r[0]) + asf(r[1]);
}
__device__ __forceinline__ float tr16(float a, float b) {
  auto r = __builtin_amdgcn_permlane16_swap(asu(a), asu(b), false, false);
  return asf(r[0]) + asf(r[1]);
}
__device__ __forceinline__ float maxhi(float a) {
  a = fmaxf(a, dpp<ROR8>(a));
  auto r = __builtin_amdgcn_permlane16_swap(asu(a), asu(a), false, false);
  a = fmaxf(asf(r[0]), asf(r[1]));
  auto t = __builtin_amdgcn_permlane32_swap(asu(a), asu(a), false, false);
  return fmaxf(asf(t[0]), asf(t[1]));
}
__device__ __forceinline__ float sumhi(float x) { x += dpp<ROR8>(x); x = tr16(x, x); return tr32(x, x); }
__device__ __forceinline__ float fold4(float a0, float a1, float b0, float b1) { float x = tr16(tr32(a0, b0), tr32(a1, b1)); return x + dpp<ROR8>(x); }
__device__ __forceinline__ float fold8(const float (&y)[8], bool b3) {
  const float t0 = tr32(y[0], y[4]), t1 = tr32(y[1], y[5]), t2 = tr32(y[2], y[6]), t3 = tr32(y[3], y[7]);
  const float u0 = tr16(t0, t2), u1 = tr16(t1, t3);
  return (b3 ? u1 : u0) + dpp<ROR8>(b3 ? u0 : u1);
}
__device__ __forceinline__ f32x2 fold8to2(const float (&y)[8]) {
  const float t0 = tr32(y[0], y[4]), t1 = tr32(y[1], y[5]), t2 = tr32(y[2], y[6]), t3 = tr32(y[3], y[7]);
  return f32x2{tr16(t0, t2), tr16(t1, t3)};
}
__device__ __forceinline__ float bperm(int src_lane, float x) { return asf(__builtin_amdgcn_ds_bpermute(src_lane * 4, asu(x))); }
__device__ __forceinline__ void wg_barrier() {
  __builtin_amdgcn_fence(__ATOMIC_RELEASE, "workgroup");
  __builtin_amdgcn_s_barrier();
  __builtin_amdgcn_fence(__ATOMIC_ACQUIRE, "workgroup");
}
__device__ __forceinline__ rsrc_t mk(const void* p, int bytes) { return __builtin_amdgcn_make_buffer_rsrc((void*)p, 0, bytes, RSRC_W3); }
#define ld128(r, voff, soff, aux) __builtin_amdgcn_raw_buffer_load_b128((r), (voff), (soff), (aux))
__device__ __forceinline__ unsigned stale(u32x4 g, unsigned tag) { return (g[1] ^ tag) | (g[3] ^ tag); }
__device__ __forceinline__ bool any(unsigned v) { return __builtin_amdgcn_ballot_w64(v != 0u) != 0ull; }
#define SB() __builtin_amdgcn_sched_barrier(0)
#define PIN8(a) asm volatile("" : "+v"(a[0]), "+v"(a[1]), "+v"(a[2]), "+v"(a[3]), "+v"(a[4]), "+v"(a[5]), "+v"(a[6]), "+v"(a[7]))
#define RELOAD() asm volatile("" ::: "memory")

__global__ void __launch_bounds__(256, 1)
fused_decode(const void* a_x, const void* a_k, const void* a_v, const void* a_wq, const void* a_wo, const void* a_wg,
             const void* a_wu, const void* a_wd, const void* a_gw, void* a_out, void* a_ws) {
  u64x8 ka0; u64x2 ka1; unsigned long long ka2, tentry, pc; unsigned code, ctmp;
  asm volatile(
      "s_load_dwordx16 %[a0], %[ka], 0x0\n\t"
      "s_memrealtime %[t]\n\t"
      "s_getpc_b64 %[pc]\n"
      ".Lcode_from:\n\t"
      "s_lshr_b32 %[c], %[wg], 3\n\t"
      "s_lshl_b32 %[c], %[c], 10\n\t"
      "v_lshl_add_u32 %[v], %[tid], 2, %[c]\n\t"
      "v_min_u32 %[v], .Lcode_to-.Lcode_from-4, %[v]\n\t"
      "global_load_dword %[v], %[v], %[pc]\n\t"
      "s_load_dwordx4 %[a1], %[ka], 0x40\n\t"
      "s_load_dwordx2 %[a2], %[ka], 0x50"
      : [a0] "=&s"(ka0), [a1] "=&s"(ka1), [a2] "=&s"(ka2), [t] "=&s"(tentry), [pc] "=&s"(pc), [c] "=&s"(ctmp), [v] "=&v"(code)
      : [ka] "s"(__builtin_amdgcn_kernarg_segment_ptr()), [wg] "s"(blockIdx.x), [tid] "v"(threadIdx.x)
      : "scc", "memory");
  extern __shared__ __attribute__((aligned(16))) float lds[];
  float* const redQ = lds;
  float* const redG = lds + 512;
  float* const redA = lds + 1024;
  unsigned short* const qs = (unsigned short*)(lds + 1536);
  float* const sml = lds + 1600;
  float* const redY = lds + 1664;

  const int b = blockIdx.x, h = (b & 7) * 4 + (b >> 6), s = (b >> 3) & 7;
  const int me = (h * 8 + s) * 1024;
  const int tid = threadIdx.x, lane = tid & 63, w = __builtin_amdgcn_readfirstlane(tid >> 6);
  const int r = lane >> 4, c = lane & 15, rho = lane >> 3, kap = lane & 7;
  const bool b0 = lane & 1, b1 = lane & 2, b2 = lane & 4, b3 = lane & 8;
  const int row0 = 64 * s + 16 * w;
  int wq_v = r * 4 * 8192 + c * 16, wq_s = row0 * 8192 + h * 256;
  asm volatile("s_waitcnt lgkmcnt(0)" : "+s"(ka0), "+s"(ka1), "+s"(ka2), "+s"(tentry), "+v"(wq_v), "+s"(wq_s) :: "memory");
  unsigned char* const ws = (unsigned char*)ka2;
  const size_t kvb = (size_t)(h * SK + s * 1024) * 256;
  const rsrc_t rs = mk(ws, WS_END), rx = mk((const void*)ka0[0], D * 2);
  const rsrc_t rk = mk((const char*)ka0[1] + kvb, 1024 * 256), rv = mk((const char*)ka0[2] + kvb, 1024 * 256);
  unsigned tstart = (unsigned)tentry + TOFF;

  u32x4 kv[RING];
  unsigned stretch = 0, tag = 0;
  u32x2 lateg; int dlate = 0;
  auto issue = [&](auto J) {
    constexpr int j = J;
    if constexpr (j % 2 == 0) {
      if constexpr (j > JGA) tstart += stretch;
      int late;
      do { late = (int)((unsigned)__builtin_amdgcn_s_memrealtime() - (tstart + slot_ticks(j))); } while (late < 0);
      if constexpr (j == DJ) dlate = late;
      if constexpr (j == JL) {
        if (late > GOV_THR && w == 0) __builtin_amdgcn_raw_buffer_store_b64(u32x2{(unsigned)late, tag}, rs, lane == 0 ? WS_LATE + b * 8 : WS_END, 0, AUX_SC1);
      }
    }
    if constexpr (j == JG) lateg = __builtin_amdgcn_raw_buffer_load_b64(rs, WS_LATE + tid * 8, 0, AUX_SC1);
    if constexpr (j < 64) kv[j % RING] = ld128(rk, tid * 16, j * 4096, AUX_NT); else kv[j % RING] = ld128(rv, tid * 16, (j - 64) * 4096, AUX_NT);
    SB();
  };
  const unsigned epoch = __builtin_amdgcn_raw_buffer_load_b32(rs, WS_EPOCH, 0, AUX_SC1);
  u32x4 wqv[4];
  {
    const rsrc_t rq = mk((const void*)ka0[3], D * H * DH * 2);
#pragma unroll
    for (int j = 0; j < 4; ++j) wqv[j] = ld128(rq, wq_v, wq_s + j * 8192, AUX_NT);
  }
  const u32x2 xq = __builtin_amdgcn_raw_buffer_load_b64(rx, 8 * r, row0 * 2, 0);
  SB();
  static_for<0, JE>(issue);
  tag = epoch * 0x9E3779B1u + 0x7F4A7C15u;
  __builtin_amdgcn_raw_buffer_store_b128(u32x4{(unsigned)tentry, tag, (unsigned)tentry, tag}, rs, (b < 8 && tid < 8) ? WS_T0 + b * 128 + lane * 16 : WS_END, 0, AUX_SC1);
  SB();
  static_for<JE, JT>(issue);
  const u32x2 t0g = __builtin_amdgcn_raw_buffer_load_b64(rs, WS_T0 + kap * 128, 0, AUX_SC1);
  SB();
  static_for<JT, J0>(issue);
  {
    float qa[8];
#pragma unroll
    for (int j = 0; j < 4; ++j) {
      const float xv = (j & 1) ? hi(xq[j >> 1]) : lo(xq[j >> 1]);
#pragma unroll
      for (int k = 0; k < 4; ++k) {
        qa[2 * k] = j ? fmaf(xv, lo(wqv[j][k]), qa[2 * k]) : xv * lo(wqv[j][k]);
        qa[2 * k + 1] = j ? fmaf(xv, hi(wqv[j][k]), qa[2 * k + 1]) : xv * hi(wqv[j][k]);
      }
    }
    *(f32x2*)(redQ + w * 128 + 2 * lane) = fold8to2(qa);
  }
  SB();
  static_for<J0, J0 + 2>(issue);
  const int pub_lds = r * 128 + 2 * (16 * (c & 3) + 4 * w + (c >> 2)), pub_off = lane < 16 ? me + (16 * w + c) * 16 : WS_END;
  auto publish_pairs = [&](const float* red, int base, bool on) {
    const f32x2 a = *(const f32x2*)(red + pub_lds);
    float t = tr32(a[0], a[1]);
    t = tr16(t, t);
    auto r2 = __builtin_amdgcn_permlane32_swap(asu(t), asu(t), false, false);
    __builtin_amdgcn_raw_buffer_store_b128(u32x4{r2[0], tag, r2[1], tag}, rs, on ? pub_off : WS_END, base, AUX_SC1);
    SB();
  };
  wg_barrier();
  publish_pairs(redQ, WS_Q, true);
  static_for<J0 + 2, JT + 10>(issue);
  {
    const bool have = t0g[1] == tag;
    const float dsum = sum8(have ? (float)(int)(t0g[0] - (unsigned)tentry) : 0.f), cnt = sum8(have ? 1.f : 0.f);
    const float shift = fminf(fmaxf(dsum / fmaxf(cnt, 1.f) - (cnt > 0.f ? (float)OSHIFT : 0.f), -300.f), 100.f);
    tstart += __builtin_amdgcn_readfirstlane((int)shift);
    SB();
  }
  static_for<JT + 10, JU>(issue);
  u32x4 gv[2], uv[2];
  {
    const rsrc_t rg = mk((const void*)ka0[5], D * F * 2), ru = mk((const void*)ka0[6], D * F * 2);
    const int chunk = 6 * h + kap;
    const int go = (kap < 6 && chunk < F / 8) ? rho * (F * 2) + chunk * 16 : D * F * 2;
#pragma unroll
    for (int t = 0; t < 2; ++t) { gv[t] = ld128(rg, go, (row0 + 8 * t) * (F * 2), AUX_NT); uv[t] = ld128(ru, go, (row0 + 8 * t) * (F * 2), AUX_NT); }
  }
  const unsigned short xg0 = __builtin_amdgcn_raw_buffer_load_b16(rx, 2 * rho, row0 * 2, 0);
  const unsigned short xg1 = __builtin_amdgcn_raw_buffer_load_b16(rx, 2 * rho + 16, row0 * 2, 0);
  SB();
  static_for<JU, J1>(issue);
  const int goff = (h * 8 + rho) * 1024 + w * 256 + kap * 16;
  u32x4 g0 = ld128(rs, WS_Q + goff, 0, AUX_SC1), g1 = ld128(rs, WS_Q + goff + 128, 0, AUX_SC1);
  SB();
  static_for<J1, JC>(issue);
  {
    float p[16];
    const float x0 = lo(xg0), x1 = lo(xg1);
#pragma unroll
    for (int k = 0; k < 4; ++k) {
      p[2 * k] = fmaf(x1, lo(gv[1][k]), x0 * lo(gv[0][k])); p[2 * k + 1] = fmaf(x1, hi(gv[1][k]), x0 * hi(gv[0][k]));
      p[8 + 2 * k] = fmaf(x1, lo(uv[1][k]), x0 * lo(uv[0][k])); p[9 + 2 * k] = fmaf(x1, hi(uv[1][k]), x0 * hi(uv[0][k]));
    }
#pragma unroll
    for (int k = 0; k < 8; ++k) p[k] = tr32(p[k], p[k + 8]);
#pragma unroll
    for (int k = 0; k < 4; ++k) p[k] = tr16(p[k], p[k + 4]);
#pragma unroll
    for (int k = 0; k < 2; ++k) p[k] = (b3 ? p[k + 2] : p[k]) + dpp<ROR8>(b3 ? p[k] : p[k + 2]);
    const int slot = 8 * kap + rho;
    *(f32x2*)(redG + w * 128 + 2 * (16 * (slot & 3) + (slot >> 2))) = f32x2{p[0], p[1]};
    SB();
  }
  static_for<JC, J2>(issue);
  bool ok = true;
  asm volatile("" :: "v"(code));
  if (__builtin_expect(any(stale(g0, tag) | stale(g1, tag)), 0)) {
#pragma nounroll
    for (unsigned spins = 0;;) {
      RELOAD();
      g0 = ld128(rs, WS_Q + goff, 0, AUX_SC1); g1 = ld128(rs, WS_Q + goff + 128, 0, AUX_SC1);
      if (!any(stale(g0, tag) | stale(g1, tag))) break;
      if (++spins > SPIN_CAP) { ok = false; break; }
    }
  }
  {
    const float qd = fold4(asf(g0[0]), asf(g0[2]), asf(g1[0]), asf(g1[2]));
    qs[32 * w + (lane >> 5) * 16 + 2 * kap + (r & 1)] = (unsigned short)pkbf(qd, qd);
  }
  wg_barrier();
  const u32x4 qv = *(const u32x4*)(qs + 8 * c);
  publish_pairs(redG, WS_GU, w < 3);

  float sc[4], f1, f2, f4, f8;
  auto eat_k = [&](auto I) {
    constexpr int i = I;
    const u32x4 k = kv[i % RING];
    float d = dot2(k[0], qv[0], 0.f); d = dot2(k[1], qv[1], d); d = dot2(k[2], qv[2], d); d = dot2(k[3], qv[3], d);
    if constexpr (!(i & 1)) f1 = d;
    else {
      d = (b0 ? d : f1) + dpp<XOR1>(b0 ? f1 : d);
      if constexpr (!(i & 2)) f2 = d;
      else {
        d = (b1 ? d : f2) + dpp<XOR2>(b1 ? f2 : d);
        if constexpr (!(i & 4)) f4 = d;
        else {
          const float t1 = f4 + dpp<ROR12>(f4), t2 = d + dpp<ROR4>(d);
          d = b2 ? t2 : t1;
          if constexpr (!(i & 8)) f8 = d;
          else { d = (b3 ? d : f8) + dpp<ROR8>(b3 ? f8 : d); sc[i >> 4] = rbf(rbf(d) * 0.08838834764831845f); }
        }
      }
    }
    asm volatile("" : "+v"(d));
    SB();
  };
  constexpr int JK = first_slot_with(64);
  static_assert(JE < JT && JT <= J0 && J0 + 2 <= JT + 10 && JT + 10 <= JU && JU <= J1 && J1 <= JC && JC <= J2 && cons(JK) == 64 && LEAD < RING && LEND >= 6 && JK + 3 < JWARM && JWARM + LEAD < 128, "schedule");
  static_for<J2, JK + 1>([&](auto J) { constexpr int j = J; issue(J); static_for<cons(j - 1), cons(j)>(eat_k); });
  const float m = fmaxf(maxhi(max8(fmaxf(fmaxf(sc[0], sc[1]), fmaxf(sc[2], sc[3])))), -3.0e38f);
#pragma unroll
  for (int i = 0; i < 4; ++i) sc[i] = expd(sc[i], m);
  const float l = sumhi(sum8((sc[0] + sc[1]) + (sc[2] + sc[3])));
  const float lsum = sumhi(sum8(lateg[1] == tag ? (float)(int)lateg[0] : 0.f));
  if (lane == 0) { *(f32x2*)(sml + 2 * w) = f32x2{m, l}; sml[8 + w] = lsum; }

  float acc[8] = {0.f, 0.f, 0.f, 0.f, 0.f, 0.f, 0.f, 0.f};
  auto eat_v = [&](auto I) {
    constexpr int i = I - 64;
    const u32x4 v = kv[(64 + i) % RING];
    const float p = dpp<BCAST + (i & 15)>(sc[i >> 4]);
#pragma unroll
    for (int k = 0; k < 4; ++k) { acc[2 * k] = fmaf(p, lo(v[k]), acc[2 * k]); acc[2 * k + 1] = fmaf(p, hi(v[k]), acc[2 * k + 1]); }
    PIN8(acc);
    SB();
  };
  u32x4 wt8[6], warm;
  const rsrc_t ro = mk((const void*)ka0[4], H * DH * D * 2), rd = mk((const void*)ka0[7], F * D * 2);
  const int dro = (2 * (rho & 3) + (rho >> 2)) * 1024 + kap * 16 + s * 128;
  auto tile = [&](auto T) {
    constexpr int t = T;
    if constexpr (t < 4) wt8[t] = ld128(ro, rho * 2048 + kap * 16, (h * 128 + 32 * w + 16 * (t >> 1) + (t & 1)) * 1024 + s * 128, AUX_NT);
    else if constexpr (t == 4) wt8[4] = ld128(rd, dro + (6 * h + w) * 8192, 0, AUX_NT);
    else wt8[5] = ld128(rd, w < 2 ? dro + (6 * h + w + 4) * 8192 : F * D * 2, 0, AUX_NT);
    SB();
  };
  auto step_v = [&](auto J) {
    constexpr int j = J;
    issue(J);
    if constexpr (j == JWARM) {
      warm = ld128(rs, r == 0 ? WS_ACC + me + w * 256 + c * 16 : r == 1 ? WS_Y + me + w * 256 + c * 16 : WS_END, 0, AUX_SC1);
      SB();
    }
    static_for<cons(j - 1), cons(j)>(eat_v);
  };
  static_for<JK + 1, JK + 3>(step_v);
  wg_barrier();
  float fw;
  {
    const f32x2 ml = *(const f32x2*)(sml + 2 * (lane & 3));
    float M = ml[0];
    M = fmaxf(M, dpp<XOR1>(M)); M = fmaxf(M, dpp<XOR2>(M));
    fw = expd(m, M);
    float L = ml[1] * expd(ml[0], M);
    L += dpp<XOR1>(L); L += dpp<XOR2>(L);
    __builtin_amdgcn_raw_buffer_store_b128(u32x4{asu(M), tag, asu(L), tag}, rs, tid < 8 ? WS_ML + (h * 8 + s) * 128 + tid * 16 : WS_END, 0, AUX_SC1);
    float lm = sml[8 + (lane & 3)];
    lm += dpp<XOR1>(lm); lm += dpp<XOR2>(lm);
    stretch = __builtin_amdgcn_readfirstlane((int)(fminf(fmaxf(lm - (float)GOV_FREE, 0.f) * GOV_GAIN, (float)GOV_MAX) * (2.f / (127 - JGA)) + 0.5f));
    asm volatile("" : "+v"(fw));
    SB();
  }
  static_for<JK + 3, 128>(step_v);
  asm volatile("" :: "v"(warm));
  const int gu_off = WS_GU + (h * 8 + rho) * 1024 + w * 128 + kap * 16;
  const int gu_off1 = w < 2 ? gu_off + 512 : WS_END;
  const unsigned tag1 = w < 2 ? tag : 0u;
  const int ml_off = WS_ML + (h * 8 + rho) * 128 + kap * 16;
  static_for<cons(127), cons(127) + 4>(eat_v);
  tile(std::integral_constant<int, 5>{}); tile(std::integral_constant<int, 4>{});
  const u32x4 gx = ld128(rx, w == 0 ? lane * 16 : D * 2, 0, 0), gg = ld128(mk((const void*)ka1[0], D * 2), w == 0 ? lane * 16 : D * 2, 0, 0);
  SB();
  u32x4 gu0 = ld128(rs, gu_off, 0, AUX_SC1), gu1 = ld128(rs, gu_off1, 0, AUX_SC1), mlg = ld128(rs, ml_off, 0, AUX_SC1);
  SB();
  static_for<0, 4>(tile);
  static_for<cons(127) + 4, 128>(eat_v);
  *(f32x2*)(redA + w * 128 + 2 * lane) = fold8to2(acc) * fw;
  wg_barrier();
  publish_pairs(redA, WS_ACC, true);
  if (__builtin_expect(any(stale(gu0, tag) | stale(gu1, tag1) | stale(mlg, tag)), 0)) {
#pragma nounroll
    for (unsigned spins = 0;;) {
      RELOAD();
      gu0 = ld128(rs, gu_off, 0, AUX_SC1); gu1 = ld128(rs, gu_off1, 0, AUX_SC1); mlg = ld128(rs, ml_off, 0, AUX_SC1);
      if (!any(stale(gu0, tag) | stale(gu1, tag1) | stale(mlg, tag))) break;
      if (++spins > SPIN_CAP) { ok = false; break; }
    }
  }
  float wt;
  {
    const float ms = asf(mlg[0]);
    wt = expd(ms, maxhi(ms));
    wt *= __builtin_amdgcn_rcpf(sumhi(wt * asf(mlg[2])));
    const float gu = rbf(fold4(asf(gu0[0]), asf(gu0[2]), asf(gu1[0]), asf(gu1[2])));
    const int src = (rho & 3) + 16 * (rho >> 2);
    const float h0 = rbf(bperm(src, gu) * bperm(src + 4, gu)), h1 = rbf(bperm(src + 32, gu) * bperm(src + 36, gu));
    float y[8];
#pragma unroll
    for (int k = 0; k < 4; ++k) { y[2 * k] = fmaf(h1, lo(wt8[5][k]), h0 * lo(wt8[4][k])); y[2 * k + 1] = fmaf(h1, hi(wt8[5][k]), h0 * hi(wt8[4][k])); }
    redY[256 + w * 64 + lane] = fold8(y, b3);
  }
  float wf[4][8];
#pragma unroll
  for (int t = 0; t < 4; ++t) {
#pragma unroll
    for (int k = 0; k < 4; ++k) { wf[t][2 * k] = lo(wt8[t][k]); wf[t][2 * k + 1] = hi(wt8[t][k]); }
    PIN8(wf[t]);
  }
  asm volatile("" : "+v"(wt));
  SB();
  g0 = ld128(rs, WS_ACC + goff, 0, AUX_SC1); g1 = ld128(rs, WS_ACC + goff + 128, 0, AUX_SC1);
  if (__builtin_expect(any(stale(g0, tag) | stale(g1, tag)), 0)) {
#pragma nounroll
    for (unsigned spins = 0;;) {
      RELOAD();
      const bool s0 = stale(g0, tag) != 0u, s1 = stale(g1, tag) != 0u;
      const u32x4 n0 = ld128(rs, s0 ? WS_ACC + goff : WS_END, 0, AUX_SC1), n1 = ld128(rs, s1 ? WS_ACC + goff + 128 : WS_END, 0, AUX_SC1);
      if (s0) g0 = n0;
      if (s1) g1 = n1;
      if (!any(stale(g0, tag) | stale(g1, tag))) break;
      if (++spins > SPIN_CAP) { ok = false; break; }
    }
  }
  {
    const float od = rbf(fold4(wt * asf(g0[0]), wt * asf(g0[2]), wt * asf(g1[0]), wt * asf(g1[2])));
    const float o0 = bperm(rho, od), o1 = bperm(rho + 16, od), o2 = bperm(rho + 32, od), o3 = bperm(rho + 48, od);
    float y[8];
#pragma unroll
    for (int k = 0; k < 8; ++k) y[k] = fmaf(o3, wf[3][k], fmaf(o2, wf[2][k], fmaf(o1, wf[1][k], o0 * wf[0][k])));
    const float yv = fold8(y, b3);
    redY[w * 64 + lane] = ok ? yv : __builtin_nanf("");
  }
  wg_barrier();
  if (w >= 2) { { const unsigned tgt_ = (unsigned)tentry + 4000u + 8u * (unsigned)(DEXPR); while ((int)((unsigned)__builtin_amdgcn_s_memrealtime() - tgt_) < 0) __builtin_amdgcn_s_sleep(1); } return; }
  {
    const float* p = redY + w * 256 + 8 * kap + rho;
    const float t = (p[0] + p[64]) + (p[128] + p[192]);
    __builtin_amdgcn_raw_buffer_store_b64(u32x2{asu(t), tag}, rs, WS_Y + me + w * 512 + lane * 8, 0, AUX_SC1);
  }
  if (w == 1) { { const unsigned tgt_ = (unsigned)tentry + 4000u + 8u * (unsigned)(DEXPR); while ((int)((unsigned)__builtin_amdgcn_s_memrealtime() - tgt_) < 0) __builtin_amdgcn_s_sleep(1); } return; }
  float gate = 0.f;
#pragma unroll
  for (int k = 0; k < 4; ++k) { gate += rbf(lo(gx[k]) * lo(gg[k])); gate += rbf(hi(gx[k]) * hi(gg[k])); }
  gate = rbf(sumhi(sum8(gate)));
  gate = rbf(1.f / (1.f + __expf(-gate)));
  if (lane >= 32) gate = rbf(1.f - gate);
  asm volatile("" : "+v"(gate));
  SB();
  const int y_off = WS_Y + ((lane & 31) * 8 + s) * 1024 + (lane >> 5) * 512 + h * 16;
  __builtin_amdgcn_s_sleep(12);
  g0 = ld128(rs, y_off, 0, AUX_SC1);
#pragma nounroll
  for (unsigned spins = 0; any(stale(g0, tag));) {
    RELOAD();
    const bool s0 = stale(g0, tag) != 0u;
    const u32x4 n0 = ld128(rs, s0 ? y_off : WS_END, 0, AUX_SC1);
    if (s0) g0 = n0;
    if (++spins > SPIN_CAP) { ok = false; break; }
  }
  {
    float v0 = sum8(asf(g0[0])), v1 = sum8(asf(g0[2]));
    v0 += dpp<ROR8>(v0); v1 += dpp<ROR8>(v1);
    float t = rbf(tr16(v0, v1));
    t = rbf(gate * t);
    t = tr32(t, t);
    auto r2 = __builtin_amdgcn_permlane16_swap(asu(t), asu(t), false, false);
    const unsigned o = ok ? pkbf(asf(r2[0]), asf(r2[1])) : 0x7fc07fc0u;
    __builtin_amdgcn_raw_buffer_store_b32(o, mk((const void*)ka1[1], D * 2), lane == 0 ? (32 * s + h) * 4 : D * 2, 0, 0);
    if (b == 0) __builtin_amdgcn_raw_buffer_store_b32(tag, rs, lane == 0 ? WS_EPOCH : WS_END, 0, AUX_SC1);
  }
  { const unsigned tgt_ = (unsigned)tentry + 4000u + 8u * (unsigned)(DEXPR); while ((int)((unsigned)__builtin_amdgcn_s_memrealtime() - tgt_) < 0) __builtin_amdgcn_s_sleep(1); }
  asm volatile(".Lcode_to:");
}

extern "C" void kernel_launch(void* const* d_in, const int* in_sizes, int n_in, void* d_out, int out_size, void* d_ws, size_t ws_size,
                              hipStream_t stream) {
  (void)hipFuncSetAttribute((const void*)fused_decode, hipFuncAttributeMaxDynamicSharedMemorySize, LDS_BYTES);
  fused_decode<<<256, 256, LDS_BYTES, stream>>>(d_in[0], d_in[1], d_in[2], d_in[3], d_in[4], d_in[6], d_in[7], d_in[8], d_in[9], d_out, d_ws);
}
```

```cpp
#include <hip/hip_runtime.h>
#include <hip/hip_bf16.h>
#include <cstdint>
#include <utility>

constexpr int TAU = 3686;
constexpr int TOFF = 60;
constexpr int LEAD = 16;
constexpr int LEND = 10;
constexpr int CATCH = 4;
constexpr int JE = 4, JT = 12;
constexpr int J0 = 16;
constexpr int JU = 22;
constexpr int J1 = 32;
constexpr int JC = 38;
constexpr int J2 = 44;
constexpr int JL = 48, JG = 62, JGA = 83;
constexpr int JWARM = 108;
constexpr int RING = J2 + 1;
constexpr int OSHIFT = 10, GOV_THR = 40, GOV_FREE = 1500, GOV_MAX = 90;
constexpr float GOV_GAIN = 0.025f;

template <int A, int... I, class Fn> __device__ __forceinline__ void static_for_impl(std::integer_sequence<int, I...>, Fn&& f) { (f(std::integral_constant<int, A + I>{}), ...); }
template <int A, int B, class Fn> __device__ __forceinline__ void static_for(Fn&& f) { if constexpr (B > A) static_for_impl<A>(std::make_integer_sequence<int, B - A>{}, f); }
constexpr int cmin(int a, int b) { return a < b ? a : b; }
constexpr int cmax(int a, int b) { return a > b ? a : b; }
constexpr int cons(int j) { return j < J2 ? 0 : cmin(CATCH * (j - J2 + 1), cmax(cmax(j + 1 - LEAD, 128 - LEND - 2 * (127 - j)), 0)); }
constexpr int first_slot_with(int n) { int j = 0; while (cons(j) < n) ++j; return j; }
constexpr int slot_ticks(int j) { return ((j + (j >= JU ? 3 : 0)) * TAU) >> 8; }

typedef unsigned u32x4 __attribute__((ext_vector_type(4)));
typedef unsigned u32x2 __attribute__((ext_vector_type(2)));
typedef unsigned long long u64x8 __attribute__((ext_vector_type(8)));
typedef unsigned long long u64x2 __attribute__((ext_vector_type(2)));
typedef float f32x2 __attribute__((ext_vector_type(2)));
typedef __bf16 bf16x2 __attribute__((ext_vector_type(2)));
typedef __amdgpu_buffer_rsrc_t rsrc_t;

constexpr int D = 512, H = 32, SK = 8192, DH = 128, F = 1376;
constexpr int AUX_NT = 2, AUX_SC1 = 16, RSRC_W3 = 0x00020000;
constexpr int WS_EPOCH = 0;
constexpr int WS_T0 = 1024;
constexpr int WS_LATE = 2048;
constexpr int WS_ML = 4096;
constexpr int WS_Q = 65536;
constexpr int WS_GU = WS_Q + 256 * 1024;
constexpr int WS_ACC = WS_GU + 256 * 1024;
constexpr int WS_Y = WS_ACC + 256 * 1024;
constexpr int WS_END = WS_Y + 256 * 1024;
constexpr unsigned SPIN_CAP = 1u << 18;
constexpr int LDS_BYTES = 96 * 1024;

__device__ __forceinline__ float lo(unsigned u) { return __builtin_bit_cast(float, u << 16); }
__device__ __forceinline__ float hi(unsigned u) { return __builtin_bit_cast(float, u & 0xffff0000u); }
__device__ __forceinline__ float asf(unsigned u) { return __builtin_bit_cast(float, u); }
__device__ __forceinline__ unsigned asu(float f) { return __builtin_bit_cast(unsigned, f); }
__device__ __forceinline__ unsigned pkbf(float a, float b) {
  bf16x2 v = {(__bf16)a, (__bf16)b};
  return __builtin_bit_cast(unsigned, v);
}
__device__ __forceinline__ float rbf(float f) { return lo(pkbf(f, f)); }
__device__ __forceinline__ float expd(float a, float b) { return __builtin_amdgcn_exp2f((a - b) * 1.4426950408889634f); }
__device__ __forceinline__ float dot2(unsigned k, unsigned q, float acc) {
  return __builtin_amdgcn_fdot2_f32_bf16(__builtin_bit_cast(bf16x2, k), __builtin_bit_cast(bf16x2, q), acc, false);
}
template <int CTRL> __device__ __forceinline__ int dppi(int x) { return __builtin_amdgcn_mov_dpp(x, CTRL, 0xf, 0xf, true); }
template <int CTRL> __device__ __forceinline__ float dpp(float x) { return __builtin_bit_cast(float, dppi<CTRL>(__builtin_bit_cast(int, x))); }
constexpr int XOR1 = 0xB1, XOR2 = 0x4E, XOR7 = 0x141, ROR8 = 0x128, ROR4 = 0x124, ROR12 = 0x12C, BCAST = 0x150;
__device__ __forceinline__ float sum8(float x) { x += dpp<XOR1>(x); x += dpp<XOR2>(x); x += dpp<XOR7>(x); return x; }
__device__ __forceinline__ float max8(float x) { x = fmaxf(x, dpp<XOR1>(x)); x = fmaxf(x, dpp<XOR2>(x)); x = fmaxf(x, dpp<XOR7>(x)); return x; }
__device__ __forceinline__ float tr32(float a, float b) {
  auto r = __builtin_amdgcn_permlane32_swap(asu(a), asu(b), false, false);
  return asf(r[0]) + asf(r[1]);
}
__device__ __forceinline__ float tr16(float a, float b) {
  auto r = __builtin_amdgcn_permlane16_swap(asu(a), asu(b), false, false);
  return asf(r[0]) + asf(r[1]);
}
__device__ __forceinline__ float maxhi(float a) {
  a = fmaxf(a, dpp<ROR8>(a));
  auto r = __builtin_amdgcn_permlane16_swap(asu(a), asu(a), false, false);
  a = fmaxf(asf(r[0]), asf(r[1]));
  auto t = __builtin_amdgcn_permlane32_swap(asu(a), asu(a), false, false);
  return fmaxf(asf(t[0]), asf(t[1]));
}
__device__ __forceinline__ float sumhi(float x) { x += dpp<ROR8>(x); x = tr16(x, x); return tr32(x, x); }
__device__ __forceinline__ float fold4(float a0, float a1, float b0, float b1) { float x = tr16(tr32(a0, b0), tr32(a1, b1)); return x + dpp<ROR8>(x); }
__device__ __forceinline__ float fold8(const float (&y)[8], bool b3) {
  const float t0 = tr32(y[0], y[4]), t1 = tr32(y[1], y[5]), t2 = tr32(y[2], y[6]), t3 = tr32(y[3], y[7]);
  const float u0 = tr16(t0, t2), u1 = tr16(t1, t3);
  return (b3 ? u1 : u0) + dpp<ROR8>(b3 ? u0 : u1);
}
__device__ __forceinline__ f32x2 fold8to2(const float (&y)[8]) {
  const float t0 = tr32(y[0], y[4]), t1 = tr32(y[1], y[5]), t2 = tr32(y[2], y[6]), t3 = tr32(y[3], y[7]);
  return f32x2{tr16(t0, t2), tr16(t1, t3)};
}
__device__ __forceinline__ float bperm(int src_lane, float x) { return asf(__builtin_amdgcn_ds_bpermute(src_lane * 4, asu(x))); }
__device__ __forceinline__ void wg_barrier() {
  __builtin_amdgcn_fence(__ATOMIC_RELEASE, "workgroup");
  __builtin_amdgcn_s_barrier();
  __builtin_amdgcn_fence(__ATOMIC_ACQUIRE, "workgroup");
}
__device__ __forceinline__ rsrc_t mk(const void* p, int bytes) { return __builtin_amdgcn_make_buffer_rsrc((void*)p, 0, bytes, RSRC_W3); }
#define ld128(r, voff, soff, aux) __builtin_amdgcn_raw_buffer_load_b128((r), (voff), (soff), (aux))
__device__ __forceinline__ unsigned stale(u32x4 g, unsigned tag) { return (g[1] ^ tag) | (g[3] ^ tag); }
__device__ __forceinline__ bool any(unsigned v) { return __builtin_amdgcn_ballot_w64(v != 0u) != 0ull; }
#define SB() __builtin_amdgcn_sched_barrier(0)
#define PIN8(a) asm volatile("" : "+v"(a[0]), "+v"(a[1]), "+v"(a[2]), "+v"(a[3]), "+v"(a[4]), "+v"(a[5]), "+v"(a[6]), "+v"(a[7]))
#define RELOAD() asm volatile("" ::: "memory")

__global__ void __launch_bounds__(256, 1)
fused_decode(const void* a_x, const void* a_k, const void* a_v, const void* a_wq, const void* a_wo, const void* a_wg,
             const void* a_wu, const void* a_wd, const void* a_gw, void* a_out, void* a_ws) {
  u64x8 ka0; u64x2 ka1; unsigned long long ka2, tentry, pc; unsigned code, ctmp;
  asm volatile(
      "s_load_dwordx16 %[a0], %[ka], 0x0\n\t"
      "s_memrealtime %[t]\n\t"
      "s_getpc_b64 %[pc]\n"
      ".Lcode_from:\n\t"
      "s_lshr_b32 %[c], %[wg], 3\n\t"
      "s_lshl_b32 %[c], %[c], 10\n\t"
      "v_lshl_add_u32 %[v], %[tid], 2, %[c]\n\t"
      "v_min_u32 %[v], .Lcode_to-.Lcode_from-4, %[v]\n\t"
      "global_load_dword %[v], %[v], %[pc]\n\t"
      "s_load_dwordx4 %[a1], %[ka], 0x40\n\t"
      "s_load_dwordx2 %[a2], %[ka], 0x50"
      : [a0] "=&s"(ka0), [a1] "=&s"(ka1), [a2] "=&s"(ka2), [t] "=&s"(tentry), [pc] "=&s"(pc), [c] "=&s"(ctmp), [v] "=&v"(code)
      : [ka] "s"(__builtin_amdgcn_kernarg_segment_ptr()), [wg] "s"(blockIdx.x), [tid] "v"(threadIdx.x)
      : "scc", "memory");
  extern __shared__ __attribute__((aligned(16))) float lds[];
  float* const redQ = lds;
  float* const redG = lds + 512;
  float* const redA = lds + 1024;
  unsigned short* const qs = (unsigned short*)(lds + 1536);
  float* const sml = lds + 1600;
  float* const redY = lds + 1664;

  const int b = blockIdx.x, h = (b & 7) * 4 + (b >> 6), s = (b >> 3) & 7;
  const int me = (h * 8 + s) * 1024;
  const int tid = threadIdx.x, lane = tid & 63, w = __builtin_amdgcn_readfirstlane(tid >> 6);
  const int r = lane >> 4, c = lane & 15, rho = lane >> 3, kap = lane & 7;
  const bool b0 = lane & 1, b1 = lane & 2, b2 = lane & 4, b3 = lane & 8;
  const int row0 = 64 * s + 16 * w;
  int wq_v = r * 4 * 8192 + c * 16, wq_s = row0 * 8192 + h * 256;
  asm volatile("s_waitcnt lgkmcnt(0)" : "+s"(ka0), "+s"(ka1), "+s"(ka2), "+s"(tentry), "+v"(wq_v), "+s"(wq_s) :: "memory");
  unsigned char* const ws = (unsigned char*)ka2;
  const size_t kvb = (size_t)(h * SK + s * 1024) * 256;
  const rsrc_t rs = mk(ws, WS_END), rx = mk((const void*)ka0[0], D * 2);
  const rsrc_t rk = mk((const char*)ka0[1] + kvb, 1024 * 256), rv = mk((const char*)ka0[2] + kvb, 1024 * 256);
  unsigned tstart = (unsigned)tentry + TOFF;

  u32x4 kv[RING];
  unsigned stretch = 0, tag = 0;
  u32x2 lateg;
  auto issue = [&](auto J) {
    constexpr int j = J;
    if constexpr (j % 2 == 0) {
      if constexpr (j > JGA) tstart += stretch;
      int late;
      do { late = (int)((unsigned)__builtin_amdgcn_s_memrealtime() - (tstart + slot_ticks(j))); } while (late < 0);
      if constexpr (j == JL) {
        if (late > GOV_THR && w == 0) __builtin_amdgcn_raw_buffer_store_b64(u32x2{(unsigned)late, tag}, rs, lane == 0 ? WS_LATE + b * 8 : WS_END, 0, AUX_SC1);
      }
    }
    if constexpr (j == JG) lateg = __builtin_amdgcn_raw_buffer_load_b64(rs, WS_LATE + tid * 8, 0, AUX_SC1);
    if constexpr (j < 64) kv[j % RING] = ld128(rk, tid * 16, j * 4096, AUX_NT); else kv[j % RING] = ld128(rv, tid * 16, (j - 64) * 4096, AUX_NT);
    SB();
  };
  const unsigned epoch = __builtin_amdgcn_raw_buffer_load_b32(rs, WS_EPOCH, 0, AUX_SC1);
  u32x4 wqv[4];
  {
    const rsrc_t rq = mk((const void*)ka0[3], D * H * DH * 2);
#pragma unroll
    for (int j = 0; j < 4; ++j) wqv[j] = ld128(rq, wq_v, wq_s + j * 8192, AUX_NT);
  }
  const u32x2 xq = __builtin_amdgcn_raw_buffer_load_b64(rx, 8 * r, row0 * 2, 0);
  SB();
  static_for<0, JE>(issue);
  tag = epoch * 0x9E3779B1u + 0x7F4A7C15u;
  __builtin_amdgcn_raw_buffer_store_b128(u32x4{(unsigned)tentry, tag, (unsigned)tentry, tag}, rs, (b < 8 && tid < 8) ? WS_T0 + b * 128 + lane * 16 : WS_END, 0, AUX_SC1);
  SB();
  static_for<JE, JT>(issue);
  const u32x2 t0g = __builtin_amdgcn_raw_buffer_load_b64(rs, WS_T0 + kap * 128, 0, AUX_SC1);
  SB();
  static_for<JT, J0>(issue);
  {
    float qa[8];
#pragma unroll
    for (int j = 0; j < 4; ++j) {
      const float xv = (j & 1) ? hi(xq[j >> 1]) : lo(xq[j >> 1]);
#pragma unroll
      for (int k = 0; k < 4; ++k) {
        qa[2 * k] = j ? fmaf(xv, lo(wqv[j][k]), qa[2 * k]) : xv * lo(wqv[j][k]);
        qa[2 * k + 1] = j ? fmaf(xv, hi(wqv[j][k]), qa[2 * k + 1]) : xv * hi(wqv[j][k]);
      }
    }
    *(f32x2*)(redQ + w * 128 + 2 * lane) = fold8to2(qa);
  }
  SB();
  static_for<J0, J0 + 2>(issue);
  const int pub_lds = r * 128 + 2 * (16 * (c & 3) + 4 * w + (c >> 2)), pub_off = lane < 16 ? me + (16 * w + c) * 16 : WS_END;
  auto publish_pairs = [&](const float* red, int base, bool on) {
    const f32x2 a = *(const f32x2*)(red + pub_lds);
    float t = tr32(a[0], a[1]);
    t = tr16(t, t);
    auto r2 = __builtin_amdgcn_permlane32_swap(asu(t), asu(t), false, false);
    __builtin_amdgcn_raw_buffer_store_b128(u32x4{r2[0], tag, r2[1], tag}, rs, on ? pub_off : WS_END, base, AUX_SC1);
    SB();
  };
  wg_barrier();
  publish_pairs(redQ, WS_Q, true);
  static_for<J0 + 2, JT + 10>(issue);
  {
    const bool have = t0g[1] == tag;
    const float dsum = sum8(have ? (float)(int)(t0g[0] - (unsigned)tentry) : 0.f), cnt = sum8(have ? 1.f : 0.f);
    const float shift = fminf(fmaxf(dsum / fmaxf(cnt, 1.f) - (cnt > 0.f ? (float)OSHIFT : 0.f), -300.f), 100.f);
    tstart += __builtin_amdgcn_readfirstlane((int)shift);
    SB();
  }
  static_for<JT + 10, JU>(issue);
  u32x4 gv[2], uv[2];
  {
    const rsrc_t rg = mk((const void*)ka0[5], D * F * 2), ru = mk((const void*)ka0[6], D * F * 2);
    const int chunk = 6 * h + kap;
    const int go = (kap < 6 && chunk < F / 8) ? rho * (F * 2) + chunk * 16 : D * F * 2;
#pragma unroll
    for (int t = 0; t < 2; ++t) { gv[t] = ld128(rg, go, (row0 + 8 * t) * (F * 2), AUX_NT); uv[t] = ld128(ru, go, (row0 + 8 * t) * (F * 2), AUX_NT); }
  }
  const unsigned short xg0 = __builtin_amdgcn_raw_buffer_load_b16(rx, 2 * rho, row0 * 2, 0);
  const unsigned short xg1 = __builtin_amdgcn_raw_buffer_load_b16(rx, 2 * rho + 16, row0 * 2, 0);
  SB();
  static_for<JU, J1>(issue);
  const int goff = (h * 8 + rho) * 1024 + w * 256 + kap * 16;
  u32x4 g0 = ld128(rs, WS_Q + goff, 0, AUX_SC1), g1 = ld128(rs, WS_Q + goff + 128, 0, AUX_SC1);
  SB();
  static_for<J1, JC>(issue);
  {
    float p[16];
    const float x0 = lo(xg0), x1 = lo(xg1);
#pragma unroll
    for (int k = 0; k < 4; ++k) {
      p[2 * k] = fmaf(x1, lo(gv[1][k]), x0 * lo(gv[0][k])); p[2 * k + 1] = fmaf(x1, hi(gv[1][k]), x0 * hi(gv[0][k]));
      p[8 + 2 * k] = fmaf(x1, lo(uv[1][k]), x0 * lo(uv[0][k])); p[9 + 2 * k] = fmaf(x1, hi(uv[1][k]), x0 * hi(uv[0][k]));
    }
#pragma unroll
    for (int k = 0; k < 8; ++k) p[k] = tr32(p[k], p[k + 8]);
#pragma unroll
    for (int k = 0; k < 4; ++k) p[k] = tr16(p[k], p[k + 4]);
#pragma unroll
    for (int k = 0; k < 2; ++k) p[k] = (b3 ? p[k + 2] : p[k]) + dpp<ROR8>(b3 ? p[k] : p[k + 2]);
    const int slot = 8 * kap + rho;
    *(f32x2*)(redG + w * 128 + 2 * (16 * (slot & 3) + (slot >> 2))) = f32x2{p[0], p[1]};
    SB();
  }
  static_for<JC, J2>(issue);
  bool ok = true;
  asm volatile("" :: "v"(code));
  if (__builtin_expect(any(stale(g0, tag) | stale(g1, tag)), 0)) {
#pragma nounroll
    for (unsigned spins = 0;;) {
      RELOAD();
      g0 = ld128(rs, WS_Q + goff, 0, AUX_SC1); g1 = ld128(rs, WS_Q + goff + 128, 0, AUX_SC1);
      if (!any(stale(g0, tag) | stale(g1, tag))) break;
      if (++spins > SPIN_CAP) { ok = false; break; }
    }
  }
  {
    const float qd = fold4(asf(g0[0]), asf(g0[2]), asf(g1[0]), asf(g1[2]));
    qs[32 * w + (lane >> 5) * 16 + 2 * kap + (r & 1)] = (unsigned short)pkbf(qd, qd);
  }
  wg_barrier();
  const u32x4 qv = *(const u32x4*)(qs + 8 * c);
  publish_pairs(redG, WS_GU, w < 3);

  float sc[4], f1, f2, f4, f8;
  auto eat_k = [&](auto I) {
    constexpr int i = I;
    const u32x4 k = kv[i % RING];
    float d = dot2(k[0], qv[0], 0.f); d = dot2(k[1], qv[1], d); d = dot2(k[2], qv[2], d); d = dot2(k[3], qv[3], d);
    if constexpr (!(i & 1)) f1 = d;
    else {
      d = (b0 ? d : f1) + dpp<XOR1>(b0 ? f1 : d);
      if constexpr (!(i & 2)) f2 = d;
      else {
        d = (b1 ? d : f2) + dpp<XOR2>(b1 ? f2 : d);
        if constexpr (!(i & 4)) f4 = d;
        else {
          const float t1 = f4 + dpp<ROR12>(f4), t2 = d + dpp<ROR4>(d);
          d = b2 ? t2 : t1;
          if constexpr (!(i & 8)) f8 = d;
          else { d = (b3 ? d : f8) + dpp<ROR8>(b3 ? f8 : d); sc[i >> 4] = rbf(rbf(d) * 0.08838834764831845f); }
        }
      }
    }
    asm volatile("" : "+v"(d));
    SB();
  };
  constexpr int JK = first_slot_with(64);
  static_assert(JE < JT && JT <= J0 && J0 + 2 <= JT + 10 && JT + 10 <= JU && JU <= J1 && J1 <= JC && JC <= J2 && cons(JK) == 64 && LEAD < RING && LEND >= 6 && JK + 3 < JWARM && JWARM + LEAD < 128, "schedule");
  static_for<J2, JK + 1>([&](auto J) { constexpr int j = J; issue(J); static_for<cons(j - 1), cons(j)>(eat_k); });
  const float m = fmaxf(maxhi(max8(fmaxf(fmaxf(sc[0], sc[1]), fmaxf(sc[2], sc[3])))), -3.0e38f);
#pragma unroll
  for (int i = 0; i < 4; ++i) sc[i] = expd(sc[i], m);
  const float l = sumhi(sum8((sc[0] + sc[1]) + (sc[2] + sc[3])));
  const float lsum = sumhi(sum8(lateg[1] == tag ? (float)(int)lateg[0] : 0.f));
  if (lane == 0) { *(f32x2*)(sml + 2 * w) = f32x2{m, l}; sml[8 + w] = lsum; }

  float acc[8] = {0.f, 0.f, 0.f, 0.f, 0.f, 0.f, 0.f, 0.f};
  auto eat_v = [&](auto I) {
    constexpr int i = I - 64;
    const u32x4 v = kv[(64 + i) % RING];
    const float p = dpp<BCAST + (i & 15)>(sc[i >> 4]);
#pragma unroll
    for (int k = 0; k < 4; ++k) { acc[2 * k] = fmaf(p, lo(v[k]), acc[2 * k]); acc[2 * k + 1] = fmaf(p, hi(v[k]), acc[2 * k + 1]); }
    PIN8(acc);
    SB();
  };
  u32x4 wt8[6], warm;
  const rsrc_t ro = mk((const void*)ka0[4], H * DH * D * 2), rd = mk((const void*)ka0[7], F * D * 2);
  const int dro = (2 * (rho & 3) + (rho >> 2)) * 1024 + kap * 16 + s * 128;
  auto tile = [&](auto T) {
    constexpr int t = T;
    if constexpr (t < 4) wt8[t] = ld128(ro, rho * 2048 + kap * 16, (h * 128 + 32 * w + 16 * (t >> 1) + (t & 1)) * 1024 + s * 128, AUX_NT);
    else if constexpr (t == 4) wt8[4] = ld128(rd, dro + (6 * h + w) * 8192, 0, AUX_NT);
    else wt8[5] = ld128(rd, w < 2 ? dro + (6 * h + w + 4) * 8192 : F * D * 2, 0, AUX_NT);
    SB();
  };
  auto step_v = [&](auto J) {
    constexpr int j = J;
    issue(J);
    if constexpr (j == JWARM) {
      warm = ld128(rs, r == 0 ? WS_ACC + me + w * 256 + c * 16 : r == 1 ? WS_Y + me + w * 256 + c * 16 : WS_END, 0, AUX_SC1);
      SB();
    }
    static_for<cons(j - 1), cons(j)>(eat_v);
  };
  static_for<JK + 1, JK + 3>(step_v);
  wg_barrier();
  float fw;
  {
    const f32x2 ml = *(const f32x2*)(sml + 2 * (lane & 3));
    float M = ml[0];
    M = fmaxf(M, dpp<XOR1>(M)); M = fmaxf(M, dpp<XOR2>(M));
    fw = expd(m, M);
    float L = ml[1] * expd(ml[0], M);
    L += dpp<XOR1>(L); L += dpp<XOR2>(L);
    __builtin_amdgcn_raw_buffer_store_b128(u32x4{asu(M), tag, asu(L), tag}, rs, tid < 8 ? WS_ML + (h * 8 + s) * 128 + tid * 16 : WS_END, 0, AUX_SC1);
    float lm = sml[8 + (lane & 3)];
    lm += dpp<XOR1>(lm); lm += dpp<XOR2>(lm);
    stretch = __builtin_amdgcn_readfirstlane((int)(fminf(fmaxf(lm - (float)GOV_FREE, 0.f) * GOV_GAIN, (float)GOV_MAX) * (2.f / (127 - JGA)) + 0.5f));
    asm volatile("" : "+v"(fw));
    SB();
  }
  static_for<JK + 3, 128>(step_v);
  asm volatile("" :: "v"(warm));
  const int gu_off = WS_GU + (h * 8 + rho) * 1024 + w * 128 + kap * 16;
  const int gu_off1 = w < 2 ? gu_off + 512 : WS_END;
  const unsigned tag1 = w < 2 ? tag : 0u;
  const int ml_off = WS_ML + (h * 8 + rho) * 128 + kap * 16;
  static_for<cons(127), cons(127) + 4>(eat_v);
  tile(std::integral_constant<int, 5>{}); tile(std::integral_constant<int, 4>{});
  const u32x4 gx = ld128(rx, w == 0 ? lane * 16 : D * 2, 0, 0), gg = ld128(mk((const void*)ka1[0], D * 2), w == 0 ? lane * 16 : D * 2, 0, 0);
  SB();
  u32x4 gu0 = ld128(rs, gu_off, 0, AUX_SC1), gu1 = ld128(rs, gu_off1, 0, AUX_SC1), mlg = ld128(rs, ml_off, 0, AUX_SC1);
  SB();
  static_for<0, 4>(tile);
  static_for<cons(127) + 4, 128>(eat_v);
  *(f32x2*)(redA + w * 128 + 2 * lane) = fold8to2(acc) * fw;
  wg_barrier();
  publish_pairs(redA, WS_ACC, true);
  if (__builtin_expect(any(stale(gu0, tag) | stale(gu1, tag1) | stale(mlg, tag)), 0)) {
#pragma nounroll
    for (unsigned spins = 0;;) {
      RELOAD();
      gu0 = ld128(rs, gu_off, 0, AUX_SC1); gu1 = ld128(rs, gu_off1, 0, AUX_SC1); mlg = ld128(rs, ml_off, 0, AUX_SC1);
      if (!any(stale(gu0, tag) | stale(gu1, tag1) | stale(mlg, tag))) break;
      if (++spins > SPIN_CAP) { ok = false; break; }
    }
  }
  float wt;
  {
    const float ms = asf(mlg[0]);
    wt = expd(ms, maxhi(ms));
    wt *= __builtin_amdgcn_rcpf(sumhi(wt * asf(mlg[2])));
    const float gu = rbf(fold4(asf(gu0[0]), asf(gu0[2]), asf(gu1[0]), asf(gu1[2])));
    const int src = (rho & 3) + 16 * (rho >> 2);
    const float h0 = rbf(bperm(src, gu) * bperm(src + 4, gu)), h1 = rbf(bperm(src + 32, gu) * bperm(src + 36, gu));
    float y[8];
#pragma unroll
    for (int k = 0; k < 4; ++k) { y[2 * k] = fmaf(h1, lo(wt8[5][k]), h0 * lo(wt8[4][k])); y[2 * k + 1] = fmaf(h1, hi(wt8[5][k]), h0 * hi(wt8[4][k])); }
    redY[256 + w * 64 + lane] = fold8(y, b3);
  }
  float wf[4][8];
#pragma unroll
  for (int t = 0; t < 4; ++t) {
#pragma unroll
    for (int k = 0; k < 4; ++k) { wf[t][2 * k] = lo(wt8[t][k]); wf[t][2 * k + 1] = hi(wt8[t][k]); }
    PIN8(wf[t]);
  }
  asm volatile("" : "+v"(wt));
  SB();
  g0 = ld128(rs, WS_ACC + goff, 0, AUX_SC1); g1 = ld128(rs, WS_ACC + goff + 128, 0, AUX_SC1);
  if (__builtin_expect(any(stale(g0, tag) | stale(g1, tag)), 0)) {
#pragma nounroll
    for (unsigned spins = 0;;) {
      RELOAD();
      const bool s0 = stale(g0, tag) != 0u, s1 = stale(g1, tag) != 0u;
      const u32x4 n0 = ld128(rs, s0 ? WS_ACC + goff : WS_END, 0, AUX_SC1), n1 = ld128(rs, s1 ? WS_ACC + goff + 128 : WS_END, 0, AUX_SC1);
      if (s0) g0 = n0;
      if (s1) g1 = n1;
      if (!any(stale(g0, tag) | stale(g1, tag))) break;
      if (++spins > SPIN_CAP) { ok = false; break; }
    }
  }
  {
    const float od = rbf(fold4(wt * asf(g0[0]), wt * asf(g0[2]), wt * asf(g1[0]), wt * asf(g1[2])));
    const float o0 = bperm(rho, od), o1 = bperm(rho + 16, od), o2 = bperm(rho + 32, od), o3 = bperm(rho + 48, od);
    float y[8];
#pragma unroll
    for (int k = 0; k < 8; ++k) y[k] = fmaf(o3, wf[3][k], fmaf(o2, wf[2][k], fmaf(o1, wf[1][k], o0 * wf[0][k])));
    const float yv = fold8(y, b3);
    redY[w * 64 + lane] = ok ? yv : __builtin_nanf("");
  }
  wg_barrier();
  if (w >= 2) return;
  {
    const float* p = redY + w * 256 + 8 * kap + rho;
    const float t = (p[0] + p[64]) + (p[128] + p[192]);
    __builtin_amdgcn_raw_buffer_store_b64(u32x2{asu(t), tag}, rs, WS_Y + me + w * 512 + lane * 8, 0, AUX_SC1);
  }
  if (w == 1) return;
  float gate = 0.f;
#pragma unroll
  for (int k = 0; k < 4; ++k) { gate += rbf(lo(gx[k]) * lo(gg[k])); gate += rbf(hi(gx[k]) * hi(gg[k])); }
  gate = rbf(sumhi(sum8(gate)));
  gate = rbf(1.f / (1.f + __expf(-gate)));
  if (lane >= 32) gate = rbf(1.f - gate);
  asm volatile("" : "+v"(gate));
  SB();
  const int y_off = WS_Y + ((lane & 31) * 8 + s) * 1024 + (lane >> 5) * 512 + h * 16;
  __builtin_amdgcn_s_sleep(12);
  g0 = ld128(rs, y_off, 0, AUX_SC1);
#pragma nounroll
  for (unsigned spins = 0; any(stale(g0, tag));) {
    RELOAD();
    const bool s0 = stale(g0, tag) != 0u;
    const u32x4 n0 = ld128(rs, s0 ? y_off : WS_END, 0, AUX_SC1);
    if (s0) g0 = n0;
    if (++spins > SPIN_CAP) { ok = false; break; }
  }
  {
    float v0 = sum8(asf(g0[0])), v1 = sum8(asf(g0[2]));
    v0 += dpp<ROR8>(v0); v1 += dpp<ROR8>(v1);
    float t = rbf(tr16(v0, v1));
    t = rbf(gate * t);
    t = tr32(t, t);
    auto r2 = __builtin_amdgcn_permlane16_swap(asu(t), asu(t), false, false);
    const unsigned o = ok ? pkbf(asf(r2[0]), asf(r2[1])) : 0x7fc07fc0u;
    __builtin_amdgcn_raw_buffer_store_b32(o, mk((const void*)ka1[1], D * 2), lane == 0 ? (32 * s + h) * 4 : D * 2, 0, 0);
    if (b == 0) __builtin_amdgcn_raw_buffer_store_b32(tag, rs, lane == 0 ? WS_EPOCH : WS_END, 0, AUX_SC1);
  }
  asm volatile(".Lcode_to:");
}

extern "C" void kernel_launch(void* const* d_in, const int* in_sizes, int n_in, void* d_out, int out_size, void* d_ws, size_t ws_size,
                              hipStream_t stream) {
  (void)hipFuncSetAttribute((const void*)fused_decode, hipFuncAttributeMaxDynamicSharedMemorySize, LDS_BYTES);
  fused_decode<<<256, 256, LDS_BYTES, stream>>>(d_in[0], d_in[1], d_in[2], d_in[3], d_in[4], d_in[6], d_in[7], d_in[8], d_in[9], d_out, d_ws);
}
```

```cpp
#include <hip/hip_runtime.h>
#include <hip/hip_bf16.h>
#include <cstdint>
#include <utility>

constexpr int TAU = 3840, TAU2 = 3560, JB = 32;
constexpr int TOFF = 60;
constexpr int LEAD = 16;
constexpr int LEND = 10;
constexpr int CATCH = 4;
constexpr int JE = 4, JT = 12;
constexpr int J0 = 16;
constexpr int JU = 22;
constexpr int J1 = 32;
constexpr int JC = 38;
constexpr int J2 = 44;
constexpr int JL1 = 32, JG1 = 46, JGA1 = 65;
constexpr int JL = 48, JG = 62, JGA = 83;
constexpr int JWARM = 108;
constexpr int RING = J2 + 1;
constexpr int OSHIFT = 10;
constexpr int GOV_THR1 = 30, GOV_FREE1 = 1000, GOV_MAX1 = 90, GOV_THR = 40, GOV_FREE = 1500, GOV_MAX = 100;
constexpr float GOV_GAIN1 = 0.04f, GOV_GAIN = 0.025f;

template <int A, int... I, class Fn> __device__ __forceinline__ void static_for_impl(std::integer_sequence<int, I...>, Fn&& f) { (f(std::integral_constant<int, A + I>{}), ...); }
template <int A, int B, class Fn> __device__ __forceinline__ void static_for(Fn&& f) { if constexpr (B > A) static_for_impl<A>(std::make_integer_sequence<int, B - A>{}, f); }
constexpr int cmin(int a, int b) { return a < b ? a : b; }
constexpr int cmax(int a, int b) { return a > b ? a : b; }
constexpr int cons(int j) { return j < J2 ? 0 : cmin(CATCH * (j - J2 + 1), cmax(cmax(j + 1 - LEAD, 128 - LEND - 2 * (127 - j)), 0)); }
constexpr int first_slot_with(int n) { int j = 0; while (cons(j) < n) ++j; return j; }
constexpr int piece_off(int j) { return (j & 3) << 10 | ((j >> 2) & 7) << 14 | (j >> 5) << 20; }
constexpr int slot_ticks(int j) { return ((j + (j >= JU ? 3 : 0)) * TAU + (j > JB ? (j - JB) * (TAU2 - TAU) : 0)) >> 8; }

typedef unsigned u32x4 __attribute__((ext_vector_type(4)));
typedef unsigned u32x2 __attribute__((ext_vector_type(2)));
typedef unsigned long long u64x8 __attribute__((ext_vector_type(8)));
typedef unsigned long long u64x2 __attribute__((ext_vector_type(2)));
typedef float f32x2 __attribute__((ext_vector_type(2)));
typedef __bf16 bf16x2 __attribute__((ext_vector_type(2)));
typedef __amdgpu_buffer_rsrc_t rsrc_t;

constexpr int D = 512, H = 32, SK = 8192, DH = 128, F = 1376;
constexpr int AUX_NT = 2, AUX_SC1 = 16, RSRC_W3 = 0x00020000;
constexpr int WS_EPOCH = 0;
constexpr int WS_T0 = 1024;
constexpr int WS_LATE = 2048;
constexpr int WS_LATE1 = 40960;
constexpr int WS_ML = 4096;
constexpr int WS_Q = 65536;
constexpr int WS_GU = WS_Q + 256 * 1024;
constexpr int WS_ACC = WS_GU + 256 * 1024;
constexpr int WS_Y = WS_ACC + 256 * 1024;
constexpr int WS_END = WS_Y + 256 * 1024;
constexpr unsigned SPIN_CAP = 1u << 18;
constexpr int LDS_BYTES = 96 * 1024;

__device__ __forceinline__ float lo(unsigned u) { return __builtin_bit_cast(float, u << 16); }
__device__ __forceinline__ float hi(unsigned u) { return __builtin_bit_cast(float, u & 0xffff0000u); }
__device__ __forceinline__ float asf(unsigned u) { return __builtin_bit_cast(float, u); }
__device__ __forceinline__ unsigned asu(float f) { return __builtin_bit_cast(unsigned, f); }
__device__ __forceinline__ unsigned pkbf(float a, float b) {
  bf16x2 v = {(__bf16)a, (__bf16)b};
  return __builtin_bit_cast(unsigned, v);
}
__device__ __forceinline__ float rbf(float f) { return lo(pkbf(f, f)); }
__device__ __forceinline__ float expd(float a, float b) { return __builtin_amdgcn_exp2f((a - b) * 1.4426950408889634f); }
__device__ __forceinline__ float dot2(unsigned k, unsigned q, float acc) {
  return __builtin_amdgcn_fdot2_f32_bf16(__builtin_bit_cast(bf16x2, k), __builtin_bit_cast(bf16x2, q), acc, false);
}
template <int CTRL> __device__ __forceinline__ int dppi(int x) { return __builtin_amdgcn_mov_dpp(x, CTRL, 0xf, 0xf, true); }
template <int CTRL> __device__ __forceinline__ float dpp(float x) { return __builtin_bit_cast(float, dppi<CTRL>(__builtin_bit_cast(int, x))); }
constexpr int XOR1 = 0xB1, XOR2 = 0x4E, XOR7 = 0x141, ROR8 = 0x128, ROR4 = 0x124, ROR12 = 0x12C, BCAST = 0x150;
__device__ __forceinline__ float sum8(float x) { x += dpp<XOR1>(x); x += dpp<XOR2>(x); x += dpp<XOR7>(x); return x; }
__device__ __forceinline__ float max8(float x) { x = fmaxf(x, dpp<XOR1>(x)); x = fmaxf(x, dpp<XOR2>(x)); x = fmaxf(x, dpp<XOR7>(x)); return x; }
__device__ __forceinline__ float tr32(float a, float b) {
  auto r = __builtin_amdgcn_permlane32_swap(asu(a), asu(b), false, false);
  return asf(r[0]) + asf(r[1]);
}
__device__ __forceinline__ float tr16(float a, float b) {
  auto r = __builtin_amdgcn_permlane16_swap(asu(a), asu(b), false, false);
  return asf(r[0]) + asf(r[1]);
}
__device__ __forceinline__ float maxhi(float a) {
  a = fmaxf(a, dpp<ROR8>(a));
  auto r = __builtin_amdgcn_permlane16_swap(asu(a), asu(a), false, false);
  a = fmaxf(asf(r[0]), asf(r[1]));
  auto t = __builtin_amdgcn_permlane32_swap(asu(a), asu(a), false, false);
  return fmaxf(asf(t[0]), asf(t[1]));
}
__device__ __forceinline__ float sumhi(float x) { x += dpp<ROR8>(x); x = tr16(x, x); return tr32(x, x); }
__device__ __forceinline__ float fold4(float a0, float a1, float b0, float b1) { float x = tr16(tr32(a0, b0), tr32(a1, b1)); return x + dpp<ROR8>(x); }
__device__ __forceinline__ float fold8(const float (&y)[8], bool b3) {
  const float t0 = tr32(y[0], y[4]), t1 = tr32(y[1], y[5]), t2 = tr32(y[2], y[6]), t3 = tr32(y[3], y[7]);
  const float u0 = tr16(t0, t2), u1 = tr16(t1, t3);
  return (b3 ? u1 : u0) + dpp<ROR8>(b3 ? u0 : u1);
}
__device__ __forceinline__ f32x2 fold8to2(const float (&y)[8]) {
  const float t0 = tr32(y[0], y[4]), t1 = tr32(y[1], y[5]), t2 = tr32(y[2], y[6]), t3 = tr32(y[3], y[7]);
  return f32x2{tr16(t0, t2), tr16(t1, t3)};
}
__device__ __forceinline__ float bperm(int src_lane, float x) { return asf(__builtin_amdgcn_ds_bpermute(src_lane * 4, asu(x))); }
__device__ __forceinline__ void wg_barrier() {
  __builtin_amdgcn_fence(__ATOMIC_RELEASE, "workgroup");
  __builtin_amdgcn_s_barrier();
  __builtin_amdgcn_fence(__ATOMIC_ACQUIRE, "workgroup");
}
__device__ __forceinline__ rsrc_t mk(const void* p, int bytes) { return __builtin_amdgcn_make_buffer_rsrc((void*)p, 0, bytes, RSRC_W3); }
#define ld128(r, voff, soff, aux) __builtin_amdgcn_raw_buffer_load_b128((r), (voff), (soff), (aux))
__device__ __forceinline__ unsigned stale(u32x4 g, unsigned tag) { return (g[1] ^ tag) | (g[3] ^ tag); }
__device__ __forceinline__ bool any(unsigned v) { return __builtin_amdgcn_ballot_w64(v != 0u) != 0ull; }
#define SB() __builtin_amdgcn_sched_barrier(0)
#define PIN8(a) asm volatile("" : "+v"(a[0]), "+v"(a[1]), "+v"(a[2]), "+v"(a[3]), "+v"(a[4]), "+v"(a[5]), "+v"(a[6]), "+v"(a[7]))
#define RELOAD() asm volatile("" ::: "memory")

__global__ void __launch_bounds__(256, 1)
fused_decode(const void* a_x, const void* a_k, const void* a_v, const void* a_wq, const void* a_wo, const void* a_wg,
             const void* a_wu, const void* a_wd, const void* a_gw, void* a_out, void* a_ws) {
  u64x8 ka0; u64x2 ka1; unsigned long long ka2, tentry, pc; unsigned code, ctmp;
  asm volatile(
      "s_load_dwordx16 %[a0], %[ka], 0x0\n\t"
      "s_memrealtime %[t]\n\t"
      "s_getpc_b64 %[pc]\n"
      ".Lcode_from:\n\t"
      "s_lshr_b32 %[c], %[wg], 3\n\t"
      "s_lshl_b32 %[c], %[c], 10\n\t"
      "v_lshl_add_u32 %[v], %[tid], 2, %[c]\n\t"
      "v_min_u32 %[v], .Lcode_to-.Lcode_from-4, %[v]\n\t"
      "global_load_dword %[v], %[v], %[pc]\n\t"
      "s_load_dwordx4 %[a1], %[ka], 0x40\n\t"
      "s_load_dwordx2 %[a2], %[ka], 0x50"
      : [a0] "=&s"(ka0), [a1] "=&s"(ka1), [a2] "=&s"(ka2), [t] "=&s"(tentry), [pc] "=&s"(pc), [c] "=&s"(ctmp), [v] "=&v"(code)
      : [ka] "s"(__builtin_amdgcn_kernarg_segment_ptr()), [wg] "s"(blockIdx.x), [tid] "v"(threadIdx.x)
      : "scc", "memory");
  extern __shared__ __attribute__((aligned(16))) float lds[];
  float* const redQ = lds;
  float* const redG = lds + 512;
  float* const redA = lds + 1024;
  unsigned short* const qs = (unsigned short*)(lds + 1536);
  float* const sml = lds + 1600;
  float* const redY = lds + 1664;

  const int b = blockIdx.x, h = (b & 7) * 4 + (b >> 6), s = (b >> 3) & 7;
  const int me = (h * 8 + s) * 1024;
  const int tid = threadIdx.x, lane = tid & 63, w = __builtin_amdgcn_readfirstlane(tid >> 6);
  const int r = lane >> 4, c = lane & 15, rho = lane >> 3, kap = lane & 7;
  const bool b0 = lane & 1, b1 = lane & 2, b2 = lane & 4, b3 = lane & 8;
  const int row0 = 64 * s + 16 * w;
  int wq_v = r * 4 * 8192 + c * 16, wq_s = row0 * 8192 + h * 256;
  asm volatile("s_waitcnt lgkmcnt(0)" : "+s"(ka0), "+s"(ka1), "+s"(ka2), "+s"(tentry), "+v"(wq_v), "+s"(wq_s) :: "memory");
  unsigned char* const ws = (unsigned char*)ka2;
  const size_t kvb = (size_t)h * SK * 256;
  const int kvo = lane * 16 + w * 4096 + s * 131072;
  const rsrc_t rs = mk(ws, WS_END), rx = mk((const void*)ka0[0], D * 2);
  const rsrc_t rk = mk((const char*)ka0[1] + kvb, SK * 256), rv = mk((const char*)ka0[2] + kvb, SK * 256);
  unsigned tstart = (unsigned)tentry + TOFF;

  u32x4 kv[RING];
  unsigned stretch = 0, tag = 0;
  u32x2 late1, late2;
  auto issue = [&](auto J) {
    constexpr int j = J;
    if constexpr (j % 2 == 0) {
      if constexpr (j > JGA1) tstart += stretch;
      int late;
      do { late = (int)((unsigned)__builtin_amdgcn_s_memrealtime() - (tstart + slot_ticks(j))); } while (late < 0);
      if constexpr (j == JL1 || j == JL) {
        if (late > (j == JL1 ? GOV_THR1 : GOV_THR) && w == 0)
          __builtin_amdgcn_raw_buffer_store_b64(u32x2{(unsigned)late, tag}, rs, lane == 0 ? (j == JL1 ? WS_LATE1 : WS_LATE) + b * 8 : WS_END, 0, AUX_SC1);
      }
    }
    if constexpr (j == JG1) late1 = __builtin_amdgcn_raw_buffer_load_b64(rs, WS_LATE1 + tid * 8, 0, AUX_SC1);
    if constexpr (j == JGA1 - 2) {
      const float t = sumhi(sum8(late1[1] == tag ? (float)(int)late1[0] : 0.f));
      if (lane == 0) __hip_atomic_store(sml + 12 + w, t, __ATOMIC_RELAXED, __HIP_MEMORY_SCOPE_WORKGROUP);
    }
    if constexpr (j == JGA1) {
      float t = __hip_atomic_load(sml + 12 + (lane & 3), __ATOMIC_RELAXED, __HIP_MEMORY_SCOPE_WORKGROUP);
      t += dpp<XOR1>(t); t += dpp<XOR2>(t);
      stretch = __builtin_amdgcn_readfirstlane((int)(fminf(fmaxf(t - (float)GOV_FREE1, 0.f) * GOV_GAIN1, (float)GOV_MAX1) * (2.f / (127 - JGA1)) + 0.5f));
    }
    if constexpr (j == JG) late2 = __builtin_amdgcn_raw_buffer_load_b64(rs, WS_LATE + tid * 8, 0, AUX_SC1);
    kv[j % RING] = ld128(j < 64 ? rk : rv, kvo, piece_off(j & 63), AUX_NT);
    SB();
  };
  const unsigned epoch = __builtin_amdgcn_raw_buffer_load_b32(rs, WS_EPOCH, 0, AUX_SC1);
  u32x4 wqv[4];
  {
    const rsrc_t rq = mk((const void*)ka0[3], D * H * DH * 2);
#pragma unroll
    for (int j = 0; j < 4; ++j) wqv[j] = ld128(rq, wq_v, wq_s + j * 8192, AUX_NT);
  }
  const u32x2 xq = __builtin_amdgcn_raw_buffer_load_b64(rx, 8 * r, row0 * 2, 0);
  SB();
  static_for<0, JE>(issue);
  tag = epoch * 0x9E3779B1u + 0x7F4A7C15u;
  __builtin_amdgcn_raw_buffer_store_b128(u32x4{(unsigned)tentry, tag, (unsigned)tentry, tag}, rs, (b < 8 && tid < 8) ? WS_T0 + b * 128 + lane * 16 : WS_END, 0, AUX_SC1);
  SB();
  static_for<JE, JT>(issue);
  const u32x2 t0g = __builtin_amdgcn_raw_buffer_load_b64(rs, WS_T0 + kap * 128, 0, AUX_SC1);
  SB();
  static_for<JT, J0>(issue);
  {
    float qa[8];
#pragma unroll
    for (int j = 0; j < 4; ++j) {
      const float xv = (j & 1) ? hi(xq[j >> 1]) : lo(xq[j >> 1]);
#pragma unroll
      for (int k = 0; k < 4; ++k) {
        qa[2 * k] = j ? fmaf(xv, lo(wqv[j][k]), qa[2 * k]) : xv * lo(wqv[j][k]);
        qa[2 * k + 1] = j ? fmaf(xv, hi(wqv[j][k]), qa[2 * k + 1]) : xv * hi(wqv[j][k]);
      }
    }
    if (lane == 0) sml[12 + w] = 0.f;
    *(f32x2*)(redQ + w * 128 + 2 * lane) = fold8to2(qa);
  }
  SB();
  static_for<J0, J0 + 2>(issue);
  const int pub_lds = r * 128 + 2 * (16 * (c & 3) + 4 * w + (c >> 2)), pub_off = lane < 16 ? me + (16 * w + c) * 16 : WS_END;
  auto publish_pairs = [&](const float* red, int base, bool on) {
    const f32x2 a = *(const f32x2*)(red + pub_lds);
    float t = tr32(a[0], a[1]);
    t = tr16(t, t);
    auto r2 = __builtin_amdgcn_permlane32_swap(asu(t), asu(t), false, false);
    __builtin_amdgcn_raw_buffer_store_b128(u32x4{r2[0], tag, r2[1], tag}, rs, on ? pub_off : WS_END, base, AUX_SC1);
    SB();
  };
  wg_barrier();
  publish_pairs(redQ, WS_Q, true);
  static_for<J0 + 2, JT + 10>(issue);
  {
    const bool have = t0g[1] == tag;
    const float dsum = sum8(have ? (float)(int)(t0g[0] - (unsigned)tentry) : 0.f), cnt = sum8(have ? 1.f : 0.f);
    const float shift = fminf(fmaxf(dsum / fmaxf(cnt, 1.f) - (cnt > 0.f ? (float)OSHIFT : 0.f), -300.f), 100.f);
    tstart += __builtin_amdgcn_readfirstlane((int)shift);
    SB();
  }
  static_for<JT + 10, JU>(issue);
  u32x4 gv[2], uv[2];
  {
    const rsrc_t rg = mk((const void*)ka0[5], D * F * 2), ru = mk((const void*)ka0[6], D * F * 2);
    const int chunk = 6 * h + kap;
    const int go = (kap < 6 && chunk < F / 8) ? rho * (F * 2) + chunk * 16 : D * F * 2;
#pragma unroll
    for (int t = 0; t < 2; ++t) { gv[t] = ld128(rg, go, (row0 + 8 * t) * (F * 2), AUX_NT); uv[t] = ld128(ru, go, (row0 + 8 * t) * (F * 2), AUX_NT); }
  }
  const unsigned short xg0 = __builtin_amdgcn_raw_buffer_load_b16(rx, 2 * rho, row0 * 2, 0);
  const unsigned short xg1 = __builtin_amdgcn_raw_buffer_load_b16(rx, 2 * rho + 16, row0 * 2, 0);
  SB();
  static_for<JU, J1>(issue);
  const int goff = (h * 8 + rho) * 1024 + w * 256 + kap * 16;
  u32x4 g0 = ld128(rs, WS_Q + goff, 0, AUX_SC1), g1 = ld128(rs, WS_Q + goff + 128, 0, AUX_SC1);
  SB();
  static_for<J1, JC>(issue);
  {
    float p[16];
    const float x0 = lo(xg0), x1 = lo(xg1);
#pragma unroll
    for (int k = 0; k < 4; ++k) {
      p[2 * k] = fmaf(x1, lo(gv[1][k]), x0 * lo(gv[0][k])); p[2 * k + 1] = fmaf(x1, hi(gv[1][k]), x0 * hi(gv[0][k]));
      p[8 + 2 * k] = fmaf(x1, lo(uv[1][k]), x0 * lo(uv[0][k])); p[9 + 2 * k] = fmaf(x1, hi(uv[1][k]), x0 * hi(uv[0][k]));
    }
#pragma unroll
    for (int k = 0; k < 8; ++k) p[k] = tr32(p[k], p[k + 8]);
#pragma unroll
    for (int k = 0; k < 4; ++k) p[k] = tr16(p[k], p[k + 4]);
#pragma unroll
    for (int k = 0; k < 2; ++k) p[k] = (b3 ? p[k + 2] : p[k]) + dpp<ROR8>(b3 ? p[k] : p[k + 2]);
    const int slot = 8 * kap + rho;
    *(f32x2*)(redG + w * 128 + 2 * (16 * (slot & 3) + (slot >> 2))) = f32x2{p[0], p[1]};
    SB();
  }
  static_for<JC, J2>(issue);
  bool ok = true;
  asm volatile("" :: "v"(code));
  if (__builtin_expect(any(stale(g0, tag) | stale(g1, tag)), 0)) {
#pragma nounroll
    for (unsigned spins = 0;;) {
      RELOAD();
      g0 = ld128(rs, WS_Q + goff, 0, AUX_SC1); g1 = ld128(rs, WS_Q + goff + 128, 0, AUX_SC1);
      if (!any(stale(g0, tag) | stale(g1, tag))) break;
      if (++spins > SPIN_CAP) { ok = false; break; }
    }
  }
  {
    const float qd = fold4(asf(g0[0]), asf(g0[2]), asf(g1[0]), asf(g1[2]));
    qs[32 * w + (lane >> 5) * 16 + 2 * kap + (r & 1)] = (unsigned short)pkbf(qd, qd);
  }
  wg_barrier();
  const u32x4 qv = *(const u32x4*)(qs + 8 * c);
  publish_pairs(redG, WS_GU, w < 3);

  float sc[4], f1, f2, f4, f8;
  auto eat_k = [&](auto I) {
    constexpr int i = I;
    const u32x4 k = kv[i % RING];
    float d = dot2(k[0], qv[0], 0.f); d = dot2(k[1], qv[1], d); d = dot2(k[2], qv[2], d); d = dot2(k[3], qv[3], d);
    if constexpr (!(i & 1)) f1 = d;
    else {
      d = (b0 ? d : f1) + dpp<XOR1>(b0 ? f1 : d);
      if constexpr (!(i & 2)) f2 = d;
      else {
        d = (b1 ? d : f2) + dpp<XOR2>(b1 ? f2 : d);
        if constexpr (!(i & 4)) f4 = d;
        else {
          const float t1 = f4 + dpp<ROR12>(f4), t2 = d + dpp<ROR4>(d);
          d = b2 ? t2 : t1;
          if constexpr (!(i & 8)) f8 = d;
          else { d = (b3 ? d : f8) + dpp<ROR8>(b3 ? f8 : d); sc[i >> 4] = rbf(rbf(d) * 0.08838834764831845f); }
        }
      }
    }
    asm volatile("" : "+v"(d));
    SB();
  };
  constexpr int JK = first_slot_with(64);
  static_assert(JE < JT && JT <= J0 && J0 + 2 <= JT + 10 && JT + 10 <= JU && JU <= J1 && J1 <= JC && JC <= J2 && cons(JK) == 64 && LEAD < RING && LEND >= 6 && JK + 3 < JWARM && JWARM + LEAD < 128, "schedule");
  static_for<J2, JK + 1>([&](auto J) { constexpr int j = J; issue(J); static_for<cons(j - 1), cons(j)>(eat_k); });
  const float m = fmaxf(maxhi(max8(fmaxf(fmaxf(sc[0], sc[1]), fmaxf(sc[2], sc[3])))), -3.0e38f);
#pragma unroll
  for (int i = 0; i < 4; ++i) sc[i] = expd(sc[i], m);
  const float l = sumhi(sum8((sc[0] + sc[1]) + (sc[2] + sc[3])));
  const float lsum = sumhi(sum8(late2[1] == tag ? (float)(int)late2[0] : 0.f));
  if (lane == 0) { *(f32x2*)(sml + 2 * w) = f32x2{m, l}; sml[8 + w] = lsum; }

  float acc[8] = {0.f, 0.f, 0.f, 0.f, 0.f, 0.f, 0.f, 0.f};
  auto eat_v = [&](auto I) {
    constexpr int i = I - 64;
    const u32x4 v = kv[(64 + i) % RING];
    const float p = dpp<BCAST + (i & 15)>(sc[i >> 4]);
#pragma unroll
    for (int k = 0; k < 4; ++k) { acc[2 * k] = fmaf(p, lo(v[k]), acc[2 * k]); acc[2 * k + 1] = fmaf(p, hi(v[k]), acc[2 * k + 1]); }
    PIN8(acc);
    SB();
  };
  u32x4 wt8[6], warm;
  const rsrc_t ro = mk((const void*)ka0[4], H * DH * D * 2), rd = mk((const void*)ka0[7], F * D * 2);
  const int dro = (2 * (rho & 3) + (rho >> 2)) * 1024 + kap * 16 + s * 128;
  auto tile = [&](auto T) {
    constexpr int t = T;
    if constexpr (t < 4) wt8[t] = ld128(ro, rho * 2048 + kap * 16, (h * 128 + 32 * w + 16 * (t >> 1) + (t & 1)) * 1024 + s * 128, AUX_NT);
    else if constexpr (t == 4) wt8[4] = ld128(rd, dro + (6 * h + w) * 8192, 0, AUX_NT);
    else wt8[5] = ld128(rd, w < 2 ? dro + (6 * h + w + 4) * 8192 : F * D * 2, 0, AUX_NT);
    SB();
  };
  auto step_v = [&](auto J) {
    constexpr int j = J;
    issue(J);
    if constexpr (j == JWARM) {
      warm = ld128(rs, r == 0 ? WS_ACC + me + w * 256 + c * 16 : r == 1 ? WS_Y + me + w * 256 + c * 16 : WS_END, 0, AUX_SC1);
      SB();
    }
    static_for<cons(j - 1), cons(j)>(eat_v);
  };
  static_for<JK + 1, JK + 3>(step_v);
  wg_barrier();
  float fw;
  {
    const f32x2 ml = *(const f32x2*)(sml + 2 * (lane & 3));
    float M = ml[0];
    M = fmaxf(M, dpp<XOR1>(M)); M = fmaxf(M, dpp<XOR2>(M));
    fw = expd(m, M);
    float L = ml[1] * expd(ml[0], M);
    L += dpp<XOR1>(L); L += dpp<XOR2>(L);
    __builtin_amdgcn_raw_buffer_store_b128(u32x4{asu(M), tag, asu(L), tag}, rs, tid < 8 ? WS_ML + (h * 8 + s) * 128 + tid * 16 : WS_END, 0, AUX_SC1);
    float lm = sml[8 + (lane & 3)];
    lm += dpp<XOR1>(lm); lm += dpp<XOR2>(lm);
    const float want = fminf(fmaxf(lm - (float)GOV_FREE, 0.f) * GOV_GAIN, (float)GOV_MAX) - (float)(stretch * ((JGA - JGA1) / 2));
    stretch = max((int)stretch, __builtin_amdgcn_readfirstlane((int)(want * (2.f / (127 - JGA)) + 0.5f)));
    asm volatile("" : "+v"(fw));
    SB();
  }
  static_for<JK + 3, 128>(step_v);
  asm volatile("" :: "v"(warm));
  const int gu_off = WS_GU + (h * 8 + rho) * 1024 + w * 128 + kap * 16;
  const int gu_off1 = w < 2 ? gu_off + 512 : WS_END;
  const unsigned tag1 = w < 2 ? tag : 0u;
  const int ml_off = WS_ML + (h * 8 + rho) * 128 + kap * 16;
  static_for<cons(127), cons(127) + 4>(eat_v);
  tile(std::integral_constant<int, 5>{}); tile(std::integral_constant<int, 4>{});
  const u32x4 gx = ld128(rx, w == 0 ? lane * 16 : D * 2, 0, 0), gg = ld128(mk((const void*)ka1[0], D * 2), w == 0 ? lane * 16 : D * 2, 0, 0);
  SB();
  u32x4 gu0 = ld128(rs, gu_off, 0, AUX_SC1), gu1 = ld128(rs, gu_off1, 0, AUX_SC1), mlg = ld128(rs, ml_off, 0, AUX_SC1);
  SB();
  static_for<0, 4>(tile);
  static_for<cons(127) + 4, 128>(eat_v);
  *(f32x2*)(redA + w * 128 + 2 * lane) = fold8to2(acc) * fw;
  wg_barrier();
  publish_pairs(redA, WS_ACC, true);
  if (__builtin_expect(any(stale(gu0, tag) | stale(gu1, tag1) | stale(mlg, tag)), 0)) {
#pragma nounroll
    for (unsigned spins = 0;;) {
      RELOAD();
      gu0 = ld128(rs, gu_off, 0, AUX_SC1); gu1 = ld128(rs, gu_off1, 0, AUX_SC1); mlg = ld128(rs, ml_off, 0, AUX_SC1);
      if (!any(stale(gu0, tag) | stale(gu1, tag1) | stale(mlg, tag))) break;
      if (++spins > SPIN_CAP) { ok = false; break; }
    }
  }
  float wt;
  {
    const float ms = asf(mlg[0]);
    wt = expd(ms, maxhi(ms));
    wt *= __builtin_amdgcn_rcpf(sumhi(wt * asf(mlg[2])));
    const float gu = rbf(fold4(asf(gu0[0]), asf(gu0[2]), asf(gu1[0]), asf(gu1[2])));
    const int src = (rho & 3) + 16 * (rho >> 2);
    const float h0 = rbf(bperm(src, gu) * bperm(src + 4, gu)), h1 = rbf(bperm(src + 32, gu) * bperm(src + 36, gu));
    float y[8];
#pragma unroll
    for (int k = 0; k < 4; ++k) { y[2 * k] = fmaf(h1, lo(wt8[5][k]), h0 * lo(wt8[4][k])); y[2 * k + 1] = fmaf(h1, hi(wt8[5][k]), h0 * hi(wt8[4][k])); }
    redY[256 + w * 64 + lane] = fold8(y, b3);
  }
  float wf[4][8];
#pragma unroll
  for (int t = 0; t < 4; ++t) {
#pragma unroll
    for (int k = 0; k < 4; ++k) { wf[t][2 * k] = lo(wt8[t][k]); wf[t][2 * k + 1] = hi(wt8[t][k]); }
    PIN8(wf[t]);
  }
  asm volatile("" : "+v"(wt));
  SB();
  g0 = ld128(rs, WS_ACC + goff, 0, AUX_SC1); g1 = ld128(rs, WS_ACC + goff + 128, 0, AUX_SC1);
  if (__builtin_expect(any(stale(g0, tag) | stale(g1, tag)), 0)) {
#pragma nounroll
    for (unsigned spins = 0;;) {
      RELOAD();
      const bool s0 = stale(g0, tag) != 0u, s1 = stale(g1, tag) != 0u;
      const u32x4 n0 = ld128(rs, s0 ? WS_ACC + goff : WS_END, 0, AUX_SC1), n1 = ld128(rs, s1 ? WS_ACC + goff + 128 : WS_END, 0, AUX_SC1);
      if (s0) g0 = n0;
      if (s1) g1 = n1;
      if (!any(stale(g0, tag) | stale(g1, tag))) break;
      if (++spins > SPIN_CAP) { ok = false; break; }
    }
  }
  {
    const float od = rbf(fold4(wt * asf(g0[0]), wt * asf(g0[2]), wt * asf(g1[0]), wt * asf(g1[2])));
    const float o0 = bperm(rho, od), o1 = bperm(rho + 16, od), o2 = bperm(rho + 32, od), o3 = bperm(rho + 48, od);
    float y[8];
#pragma unroll
    for (int k = 0; k < 8; ++k) y[k] = fmaf(o3, wf[3][k], fmaf(o2, wf[2][k], fmaf(o1, wf[1][k], o0 * wf[0][k])));
    const float yv = fold8(y, b3);
    redY[w * 64 + lane] = ok ? yv : __builtin_nanf("");
  }
  wg_barrier();
  if (w >= 2) return;
  {
    const float* p = redY + w * 256 + 8 * kap + rho;
    const float t = (p[0] + p[64]) + (p[128] + p[192]);
    __builtin_amdgcn_raw_buffer_store_b64(u32x2{asu(t), tag}, rs, WS_Y + me + w * 512 + lane * 8, 0, AUX_SC1);
  }
  if (w == 1) return;
  float gate = 0.f;
#pragma unroll
  for (int k = 0; k < 4; ++k) { gate += rbf(lo(gx[k]) * lo(gg[k])); gate += rbf(hi(gx[k]) * hi(gg[k])); }
  gate = rbf(sumhi(sum8(gate)));
  gate = rbf(1.f / (1.f + __expf(-gate)));
  if (lane >= 32) gate = rbf(1.f - gate);
  asm volatile("" : "+v"(gate));
  SB();
  const int y_off = WS_Y + ((lane & 31) * 8 + s) * 1024 + (lane >> 5) * 512 + h * 16;
  __builtin_amdgcn_s_sleep(12);
  g0 = ld128(rs, y_off, 0, AUX_SC1);
#pragma nounroll
  for (unsigned spins = 0; any(stale(g0, tag));) {
    RELOAD();
    const bool s0 = stale(g0, tag) != 0u;
    const u32x4 n0 = ld128(rs, s0 ? y_off : WS_END, 0, AUX_SC1);
    if (s0) g0 = n0;
    if (++spins > SPIN_CAP) { ok = false; break; }
  }
  {
    float v0 = sum8(asf(g0[0])), v1 = sum8(asf(g0[2]));
    v0 += dpp<ROR8>(v0); v1 += dpp<ROR8>(v1);
    float t = rbf(tr16(v0, v1));
    t = rbf(gate * t);
    t = tr32(t, t);
    auto r2 = __builtin_amdgcn_permlane16_swap(asu(t), asu(t), false, false);
    const unsigned o = ok ? pkbf(asf(r2[0]), asf(r2[1])) : 0x7fc07fc0u;
    __builtin_amdgcn_raw_buffer_store_b32(o, mk((const void*)ka1[1], D * 2), lane == 0 ? (32 * s + h) * 4 : D * 2, 0, 0);
    if (b == 0) __builtin_amdgcn_raw_buffer_store_b32(tag, rs, lane == 0 ? WS_EPOCH : WS_END, 0, AUX_SC1);
  }
  asm volatile(".Lcode_to:");
}

extern "C" void kernel_launch(void* const* d_in, const int* in_sizes, int n_in, void* d_out, int out_size, void* d_ws, size_t ws_size,
                              hipStream_t stream) {
  (void)hipFuncSetAttribute((const void*)fused_decode, hipFuncAttributeMaxDynamicSharedMemorySize, LDS_BYTES);
  fused_decode<<<256, 256, LDS_BYTES, stream>>>(d_in[0], d_in[1], d_in[2], d_in[3], d_in[4], d_in[6], d_in[7], d_in[8], d_in[9], d_out, d_ws);
}
```
